```python
import torch

def _softmax(x, axis=-1):
    return torch.softmax(x, dim=axis)
def _gelu(x):
    return torch.nn.functional.gelu(x, approximate="tanh")
def _silu(x):
    return torch.nn.functional.silu(x)
def _sigmoid(x):
    return torch.sigmoid(x)
def _layernorm(x, axis=-1, eps=1e-5):
    m = x.mean(dim=axis, keepdim=True)
    v = x.var(dim=axis, keepdim=True, unbiased=False)
    return (x - m) / torch.sqrt(v + eps)
def _scatter_add(tgt, idx, src, axis=0):
    return tgt.clone().index_add_(axis, idx, src)
def _segment_sum(src, seg, n):
    out = torch.zeros((n,) + src.shape[1:], dtype=src.dtype)
    return out.index_add_(0, seg, src)
def _pad(x, pw):
    flat = tuple(v for pair in reversed(pw) for v in pair)
    return torch.nn.functional.pad(x, flat)

kernel_name = 'standalone__hybrid_graph_tf_graphgps__graph'

def setup_inputs():

    _w = (lambda a: (lambda t: t.to(torch.bfloat16) if t.is_floating_point() else t)(torch.as_tensor(a)))
    N, deg, D = 10000, 32, 128
    E = N * deg
    src = (lambda lo, hi, *s: torch.randint(lo, hi, s))(0, N, E).to(torch.int64)
    dst = (lambda lo, hi, *s: torch.randint(lo, hi, s))(0, N, E).to(torch.int64)
    deg_cnt = torch.bincount(dst, minlength=N).to(float) + 1.0
    sc = 1.0 / float(D) ** 0.5
    return {
        "x": _w((lambda *s: torch.randn(*s, dtype=torch.bfloat16) * 0.05)(N, D)),
        "src": _w(src),
        "dst": _w(dst),
        "deg_inv": _w((1.0 / deg_cnt).to(float)),
        "w_msg": _w((lambda *s: torch.randn(*s, dtype=torch.bfloat16) * 0.2)(D, D) * sc),
        "wq": _w((lambda *s: torch.randn(*s, dtype=torch.bfloat16) * 1.6)(D, D) * sc),
        "wk": _w((lambda *s: torch.randn(*s, dtype=torch.bfloat16) * 1.6)(D, D) * sc),
        "wv": _w((lambda *s: torch.randn(*s, dtype=torch.bfloat16) * 0.2)(D, D) * sc),
        "w_out": _w((lambda *s: torch.randn(*s, dtype=torch.bfloat16) * 0.2)(D, D) * sc),
    }

def reference(x, src, dst, deg_inv, w_msg, wq, wk, wv, w_out):
    N, D = x.shape[0], x.shape[1]
    h = _layernorm(x, axis=-1)
    m_e = torch.einsum('nd,de->ne', h, w_msg)
    h_local = _segment_sum(m_e[src], dst, N) * deg_inv[:, None]
    q = torch.einsum('nd,de->ne', h, wq)
    k = torch.einsum('nd,de->ne', h, wk)
    v = torch.einsum('nd,de->ne', h, wv)
    logits = torch.einsum('id,jd->ij', q, k) / (float(D) ** 0.5)
    attn = _softmax(logits, axis=-1)
    h_global = torch.einsum('ij,jd->id', attn, v)
    y = torch.einsum('nd,de->ne', h_local + h_global, w_out)
    return x + y
```

```cpp
#include <hip/hip_runtime.h>
#include <hip/hip_bf16.h>
#include <stdint.h>

typedef unsigned short u16;
__device__ __forceinline__ float bf2f(u16 v) { return __uint_as_float(((unsigned)v) << 16); }
__device__ __forceinline__ u16 f2bf(float f) { unsigned u = __float_as_uint(f); u += 0x7fffu + ((u >> 16) & 1u); return (u16)(u >> 16); }

constexpr int D = 128;

__global__ void k_ln(const u16* x, float* h, int N) {
  int row = blockIdx.x; int t = threadIdx.x;
  __shared__ float s[128];
  float v = bf2f(x[row * D + t]);
  s[t] = v; __syncthreads();
  float m = 0; for (int i = 0; i < D; ++i) m += s[i]; m /= D;
  float var = 0; for (int i = 0; i < D; ++i) var += (s[i] - m) * (s[i] - m); var /= D;
  h[row * D + t] = (v - m) / sqrtf(var + 1e-5f);
}
__global__ void k_proj(const float* a, const u16* w, float* o, int N) {
  int row = blockIdx.x; int t = threadIdx.x;
  __shared__ float s[128];
  s[t] = a[row * D + t]; __syncthreads();
  float acc = 0; for (int d = 0; d < D; ++d) acc += s[d] * bf2f(w[d * D + t]);
  o[row * D + t] = acc;
}
__global__ void k_zero(float* p, int n) { int i = blockIdx.x * blockDim.x + threadIdx.x; if (i < n) p[i] = 0.f; }
__global__ void k_scatter(const float* me, const int* src, const int* dst, float* acc, int E) {
  int e = blockIdx.x; int t = threadIdx.x;
  atomicAdd(&acc[dst[e] * D + t], me[src[e] * D + t]);
}
__global__ __launch_bounds__(128) void k_attn(const float* q, const float* k, const float* v, const float* hl, const u16* deg_inv, float* comb, int N) {
  int row = blockIdx.x; int t = threadIdx.x;
  __shared__ float sq[128]; __shared__ float sp[128]; __shared__ float red[128];
  sq[t] = q[row * D + t] * 0.08838834764831845f; __syncthreads();
  float m = -1e30f, l = 0.f, o = 0.f;
  for (int j0 = 0; j0 < N; j0 += 128) {
    int j = j0 + t; float s = -1e30f;
    if (j < N) { s = 0; for (int d = 0; d < D; ++d) s += sq[d] * k[j * D + d]; }
    red[t] = s; __syncthreads();
    float tm = -1e30f; for (int i = 0; i < 128; ++i) tm = fmaxf(tm, red[i]);
    float mn = fmaxf(m, tm); float al = __expf(m - mn);
    float p = (j < N) ? __expf(s - mn) : 0.f;
    sp[t] = p; __syncthreads();
    float ps = 0; for (int i = 0; i < 128; ++i) ps += sp[i];
    l = l * al + ps; o *= al;
    int lim = min(128, N - j0);
    for (int i = 0; i < lim; ++i) o += sp[i] * v[(j0 + i) * D + t];
    m = mn; __syncthreads();
  }
  comb[row * D + t] = o / l + hl[row * D + t] * bf2f(deg_inv[row]);
}
__global__ void k_out(const float* y, const u16* x, u16* out, int n) { int i = blockIdx.x * blockDim.x + threadIdx.x; if (i < n) out[i] = f2bf(bf2f(x[i]) + y[i]); }

extern "C" void kernel_launch(void* const* d_in, const int* in_sizes, int n_in, void* d_out, int out_size, void* d_ws, size_t ws_size, hipStream_t stream) {
  const u16* x = (const u16*)d_in[0]; const int* src = (const int*)d_in[1]; const int* dst = (const int*)d_in[2];
  const u16* deg_inv = (const u16*)d_in[3]; const u16* wm = (const u16*)d_in[4]; const u16* wq = (const u16*)d_in[5];
  const u16* wk = (const u16*)d_in[6]; const u16* wv = (const u16*)d_in[7]; const u16* wo = (const u16*)d_in[8];
  int N = in_sizes[0] / D, E = in_sizes[1];
  float* ws = (float*)d_ws; size_t nd = (size_t)N * D;
  float *h = ws, *me = ws + nd, *q = ws + 2 * nd, *k = ws + 3 * nd, *v = ws + 4 * nd, *hl = ws + 5 * nd, *comb = ws + 6 * nd, *y = ws + 7 * nd;
  k_ln<<<N, 128, 0, stream>>>(x, h, N);
  k_proj<<<N, 128, 0, stream>>>(h, wm, me, N);
  k_proj<<<N, 128, 0, stream>>>(h, wq, q, N);
  k_proj<<<N, 128, 0, stream>>>(h, wk, k, N);
  k_proj<<<N, 128, 0, stream>>>(h, wv, v, N);
  k_zero<<<(nd + 255) / 256, 256, 0, stream>>>(hl, nd);
  k_scatter<<<E, 128, 0, stream>>>(me, src, dst, hl, E);
  k_attn<<<N, 128, 0, stream>>>(q, k, v, hl, deg_inv, comb, N);
  k_proj<<<N, 128, 0, stream>>>(comb, wo, y, N);
  k_out<<<(nd + 255) / 256, 256, 0, stream>>>(y, x, (u16*)d_out, nd);
}
```

```cpp
#include <hip/hip_runtime.h>
#include <hip/hip_bf16.h>
#include <stdint.h>
#define ATTN_ASM \
  "s_lshr_b32 s80, %[wv], 1\n\t" \
  "s_and_b32 s82, %[wv], 1\n\t" \
  "s_mul_i32 s73, s80, 49152\n\t" \
  "s_lshl_b32 s83, s82, 13\n\t" \
  "s_add_u32 s72, s73, s83\n\t" \
  "s_lshl_b32 s74, %[wv], 14\n\t" \
  "s_add_u32 s74, s74, 98304\n\t" \
  "s_lshl_b32 s75, %[wv], 12\n\t" \
  "s_add_u32 s75, s75, 81920\n\t" \
  "s_lshl_b32 s83, s82, 12\n\t" \
  "s_add_u32 s76, s73, s83\n\t" \
  "s_xor_b32 s77, %[wv], 2\n\t" \
  "s_lshl_b32 s77, s77, 14\n\t" \
  "s_add_u32 s77, s77, 98304\n\t" \
  "s_sub_u32 s78, 49152, s73\n\t" \
  "s_add_u32 s78, s78, s83\n\t" \
  "s_mul_i32 s79, %[jv], 40960\n\t" \
  "s_mul_i32 s83, s82, 20480\n\t" \
  "s_add_u32 s79, s79, s83\n\t" \
  "s_lshl_b32 s83, s80, 11\n\t" \
  "s_add_u32 s79, s79, s83\n\t" \
  "v_mbcnt_lo_u32_b32 v0, -1, 0\n\t" \
  "v_mbcnt_hi_u32_b32 v0, -1, v0\n\t" \
  "v_lshlrev_b32 v172, 4, v0\n\t" \
  "v_lshrrev_b32 v10, 1, v0\n\t" \
  "v_lshlrev_b32 v10, 3, v10\n\t" \
  "global_load_dwordx2 v[250:251], v10, %[offp]\n\t" \
  "s_add_u32 %[soff], %[soff], 12288\n\t" \
  "s_add_u32 m0, s72, 0\n\t" \
  "s_nop 0\n\t" \
  "buffer_load_dwordx4 v172, %[srd], %[soff] offen offset:0 lds\n\t" \
  "buffer_load_dwordx4 v172, %[srd], %[soff] offen offset:1024 lds\n\t" \
  "buffer_load_dwordx4 v172, %[srd], %[soff] offen offset:2048 lds\n\t" \
  "buffer_load_dwordx4 v172, %[srd], %[soff] offen offset:3072 lds\n\t" \
  "s_add_u32 %[soff], %[soff], 4096\n\t" \
  "s_add_u32 m0, s72, 4096\n\t" \
  "s_nop 0\n\t" \
  "buffer_load_dwordx4 v172, %[srd], %[soff] offen offset:0 lds\n\t" \
  "buffer_load_dwordx4 v172, %[srd], %[soff] offen offset:1024 lds\n\t" \
  "buffer_load_dwordx4 v172, %[srd], %[soff] offen offset:2048 lds\n\t" \
  "buffer_load_dwordx4 v172, %[srd], %[soff] offen offset:3072 lds\n\t" \
  "s_mul_i32 s83, s82, 49152\n\t" \
  "s_add_u32 s83, s83, 16384\n\t" \
  "s_mul_i32 s89, s80, 10240\n\t" \
  "s_add_u32 s88, s83, s89\n\t" \
  "v_add_u32 v1, s89, v172\n\t" \
  "s_add_u32 m0, s88, 0\n\t" \
  "s_nop 0\n\t" \
  "global_load_lds_dwordx4 v1, %[qbase] offset:0\n\t" \
  "global_load_lds_dwordx4 v1, %[qbase] offset:1024\n\t" \
  "global_load_lds_dwordx4 v1, %[qbase] offset:2048\n\t" \
  "global_load_lds_dwordx4 v1, %[qbase] offset:3072\n\t" \
  "s_add_u32 m0, s88, 4096\n\t" \
  "v_add_u32 v1, 0x1000, v1\n\t" \
  "global_load_lds_dwordx4 v1, %[qbase] offset:0\n\t" \
  "global_load_lds_dwordx4 v1, %[qbase] offset:1024\n\t" \
  "global_load_lds_dwordx4 v1, %[qbase] offset:2048\n\t" \
  "global_load_lds_dwordx4 v1, %[qbase] offset:3072\n\t" \
  "s_add_u32 m0, s88, 8192\n\t" \
  "v_add_u32 v1, 0x1000, v1\n\t" \
  "global_load_lds_dwordx4 v1, %[qbase] offset:0\n\t" \
  "global_load_lds_dwordx4 v1, %[qbase] offset:1024\n\t" \
  "v_mov_b32 v12, 0\n\t" \
  "v_mov_b32 v13, 0\n\t" \
  "v_mov_b32 v14, 0\n\t" \
  "v_mov_b32 v15, 0\n\t" \
  "v_mov_b32 v176, 0\n\t" \
  "v_mov_b32 v177, 0\n\t" \
  "v_mov_b32 v178, 0\n\t" \
  "v_mov_b32 v179, 0\n\t" \
  "v_mov_b32 v180, 0\n\t" \
  "v_mov_b32 v181, 0\n\t" \
  "v_mov_b32 v182, 0\n\t" \
  "v_mov_b32 v183, 0\n\t" \
  "v_mov_b32 v184, 0\n\t" \
  "v_mov_b32 v185, 0\n\t" \
  "v_mov_b32 v186, 0\n\t" \
  "v_mov_b32 v187, 0\n\t" \
  "v_mov_b32 v188, 0\n\t" \
  "v_mov_b32 v189, 0\n\t" \
  "v_mov_b32 v190, 0\n\t" \
  "v_mov_b32 v191, 0\n\t" \
  "v_mov_b32 v192, 0\n\t" \
  "v_mov_b32 v193, 0\n\t" \
  "v_mov_b32 v194, 0\n\t" \
  "v_mov_b32 v195, 0\n\t" \
  "v_mov_b32 v196, 0\n\t" \
  "v_mov_b32 v197, 0\n\t" \
  "v_mov_b32 v198, 0\n\t" \
  "v_mov_b32 v199, 0\n\t" \
  "v_mov_b32 v200, 0\n\t" \
  "v_mov_b32 v201, 0\n\t" \
  "v_mov_b32 v202, 0\n\t" \
  "v_mov_b32 v203, 0\n\t" \
  "v_mov_b32 v204, 0\n\t" \
  "v_mov_b32 v205, 0\n\t" \
  "v_mov_b32 v206, 0\n\t" \
  "v_mov_b32 v207, 0\n\t" \
  "v_add_u32 v11, s74, v172\n\t" \
  "ds_write_b128 v11, v[12:15] offset:6144\n\t" \
  "ds_write_b128 v11, v[12:15] offset:7168\n\t" \
  "ds_write_b128 v11, v[12:15] offset:8192\n\t" \
  "ds_write_b128 v11, v[12:15] offset:9216\n\t" \
  "ds_write_b128 v11, v[12:15] offset:10240\n\t" \
  "ds_write_b128 v11, v[12:15] offset:11264\n\t" \
  "ds_write_b128 v11, v[12:15] offset:12288\n\t" \
  "ds_write_b128 v11, v[12:15] offset:13312\n\t" \
  "ds_write_b128 v11, v[12:15] offset:14336\n\t" \
  "ds_write_b128 v11, v[12:15] offset:15360\n\t" \
  "v_and_b32 v1, 15, v0\n\t" \
  "v_lshrrev_b32 v2, 4, v0\n\t" \
  "v_and_b32 v3, 7, v0\n\t" \
  "v_lshlrev_b32 v3, 1, v3\n\t" \
  "v_lshlrev_b32 v4, 8, v1\n\t" \
  "v_add_u32 v4, s73, v4\n\t" \
  "v_add_u32 v5, 0, v2\n\t" \
  "v_xor_b32 v5, v5, v3\n\t" \
  "v_lshl_add_u32 v152, v5, 4, v4\n\t" \
  "v_add_u32 v5, 4, v2\n\t" \
  "v_xor_b32 v5, v5, v3\n\t" \
  "v_lshl_add_u32 v153, v5, 4, v4\n\t" \
  "v_add_u32 v5, 8, v2\n\t" \
  "v_xor_b32 v5, v5, v3\n\t" \
  "v_lshl_add_u32 v154, v5, 4, v4\n\t" \
  "v_add_u32 v5, 12, v2\n\t" \
  "v_xor_b32 v5, v5, v3\n\t" \
  "v_lshl_add_u32 v155, v5, 4, v4\n\t" \
  "v_lshrrev_b32 v3, 2, v1\n\t" \
  "v_and_b32 v5, 3, v0\n\t" \
  "v_lshl_add_u32 v6, v2, 2, v3\n\t" \
  "v_lshlrev_b32 v6, 8, v6\n\t" \
  "v_lshl_add_u32 v6, v5, 3, v6\n\t" \
  "v_add_u32 v6, s73, v6\n\t" \
  "v_and_b32 v7, 1, v2\n\t" \
  "v_lshl_add_u32 v7, v7, 2, v3\n\t" \
  "v_lshlrev_b32 v7, 5, v7\n\t" \
  "v_xor_b32 v8, 0, v7\n\t" \
  "v_add_u32 v156, v8, v6\n\t" \
  "v_xor_b32 v8, 32, v7\n\t" \
  "v_add_u32 v157, v8, v6\n\t" \
  "v_xor_b32 v8, 64, v7\n\t" \
  "v_add_u32 v158, v8, v6\n\t" \
  "v_xor_b32 v8, 96, v7\n\t" \
  "v_add_u32 v159, v8, v6\n\t" \
  "v_xor_b32 v8, 128, v7\n\t" \
  "v_add_u32 v160, v8, v6\n\t" \
  "v_xor_b32 v8, 160, v7\n\t" \
  "v_add_u32 v161, v8, v6\n\t" \
  "v_xor_b32 v8, 192, v7\n\t" \
  "v_add_u32 v162, v8, v6\n\t" \
  "v_xor_b32 v8, 224, v7\n\t" \
  "v_add_u32 v163, v8, v6\n\t" \
  "v_lshl_add_u32 v6, v3, 2, v2\n\t" \
  "v_lshlrev_b32 v6, 8, v6\n\t" \
  "v_lshl_add_u32 v6, v5, 3, v6\n\t" \
  "v_add_u32 v6, s74, v6\n\t" \
  "v_xor_b32 v8, 0, v7\n\t" \
  "v_add_u32 v164, v8, v6\n\t" \
  "v_xor_b32 v8, 32, v7\n\t" \
  "v_add_u32 v165, v8, v6\n\t" \
  "v_xor_b32 v8, 64, v7\n\t" \
  "v_add_u32 v166, v8, v6\n\t" \
  "v_xor_b32 v8, 96, v7\n\t" \
  "v_add_u32 v167, v8, v6\n\t" \
  "v_xor_b32 v8, 128, v7\n\t" \
  "v_add_u32 v168, v8, v6\n\t" \
  "v_xor_b32 v8, 160, v7\n\t" \
  "v_add_u32 v169, v8, v6\n\t" \
  "v_xor_b32 v8, 192, v7\n\t" \
  "v_add_u32 v170, v8, v6\n\t" \
  "v_xor_b32 v8, 224, v7\n\t" \
  "v_add_u32 v171, v8, v6\n\t" \
  "v_lshlrev_b32 v247, 4, v1\n\t" \
  "v_subrev_u32 v248, 14, v1\n\t" \
  "v_and_b32 v248, 31, v248\n\t" \
  "v_mov_b32 v249, 0x40004000\n\t" \
  "s_mov_b32 s84, 4096\n\t" \
  "s_mov_b32 s85, 3072\n\t" \
  "s_mov_b32 s86, 2048\n\t" \
  "s_mov_b32 s87, 1024\n\t" \
  "s_waitcnt vmcnt(18)\n\t" \
  "v_and_b32 v10, 1, v0\n\t" \
  "v_lshlrev_b32 v10, 4, v10\n\t" \
  "v_add_u32 v11, v250, v10\n\t" \
  "v_lshlrev_b32 v11, 2, v11\n\t" \
  "s_add_u32 m0, s74, 0\n\t" \
  "s_nop 0\n\t" \
  "global_load_lds_dwordx4 v11, %[sorted] offset:0\n\t" \
  "s_add_u32 m0, s74, 1008\n\t" \
  "s_nop 0\n\t" \
  "global_load_lds_dwordx4 v11, %[sorted] offset:16\n\t" \
  "s_add_u32 m0, s74, 2016\n\t" \
  "s_nop 0\n\t" \
  "global_load_lds_dwordx4 v11, %[sorted] offset:32\n\t" \
  "s_add_u32 m0, s74, 3024\n\t" \
  "s_nop 0\n\t" \
  "global_load_lds_dwordx4 v11, %[sorted] offset:48\n\t" \
  "v_cmp_lt_u32 vcc, 32, v251\n\t" \
  "v_sub_i32 v12, v251, v10 clamp\n\t" \
  "v_max_i32 v12, 0, v12\n\t" \
  "v_min_i32 v12, 16, v12\n\t" \
  "s_mov_b64 s[90:91], vcc\n\t" \
  "v_mov_b32 v13, v12\n\t" \
  "s_nop 1\n\t" \
  "v_add_u32_dpp v13, v13, v13 row_shr:1 row_mask:0xf bank_mask:0xf bound_ctrl:0\n\t" \
  "s_nop 1\n\t" \
  "v_add_u32_dpp v13, v13, v13 row_shr:2 row_mask:0xf bank_mask:0xf bound_ctrl:0\n\t" \
  "s_nop 1\n\t" \
  "v_add_u32_dpp v13, v13, v13 row_shr:4 row_mask:0xf bank_mask:0xf bound_ctrl:0\n\t" \
  "s_nop 1\n\t" \
  "v_add_u32_dpp v13, v13, v13 row_shr:8 row_mask:0xf bank_mask:0xf bound_ctrl:0\n\t" \
  "s_nop 1\n\t" \
  "v_add_u32_dpp v13, v13, v13 row_bcast:15 row_mask:0xa bank_mask:0xf\n\t" \
  "s_nop 1\n\t" \
  "v_add_u32_dpp v13, v13, v13 row_bcast:31 row_mask:0xc bank_mask:0xf\n\t" \
  "s_nop 1\n\t" \
  "v_readlane_b32 s89, v13, 63\n\t" \
  "s_cmp_lg_u64 s[90:91], 0\n\t" \
  "s_cselect_b32 %[ovf], 1, 0\n\t" \
  "s_cmp_gt_u32 s89, 512\n\t" \
  "s_cselect_b32 %[ovf], 1, %[ovf]\n\t" \
  "v_sub_u32 v13, v13, v12\n\t" \
  "v_lshlrev_b32 v13, 2, v13\n\t" \
  "v_add_u32 v251, s74, v13\n\t" \
  "v_add_u32 v251, 0x1000, v251\n\t" \
  "s_cmp_eq_u32 %[ovf], 0\n\t" \
  "s_cselect_b32 s89, -1, 0\n\t" \
  "v_and_b32 v250, s89, v12\n\t" \
  "s_getpc_b64 s[90:91]\n\t" \
  "s_lshl_b32 s89, %[pslice], 10\n\t" \
  "s_add_u32 s90, s90, s89\n\t" \
  "s_addc_u32 s91, s91, 0\n\t" \
  "global_load_dwordx4 v[240:243], v172, s[90:91]\n\t" \
  "s_waitcnt vmcnt(5) lgkmcnt(0)\n\t" \
  "s_barrier\n\t" \
  "v_add_u32 v1, s83, v172\n\t" \
  "ds_read_b128 v[120:123], v152 offset:0\n\t" \
  "ds_read_b128 v[124:127], v153 offset:0\n\t" \
  "ds_read_b128 v[128:131], v154 offset:0\n\t" \
  "ds_read_b128 v[132:135], v155 offset:0\n\t" \
  "ds_read_b128 a[160:163], v1 offset:0\n\t" \
  "ds_read_b128 a[164:167], v1 offset:1024\n\t" \
  "ds_read_b128 a[168:171], v1 offset:2048\n\t" \
  "ds_read_b128 a[172:175], v1 offset:3072\n\t" \
  "ds_read_b128 a[176:179], v1 offset:4096\n\t" \
  "ds_read_b128 a[180:183], v1 offset:5120\n\t" \
  "ds_read_b128 a[184:187], v1 offset:6144\n\t" \
  "ds_read_b128 a[188:191], v1 offset:7168\n\t" \
  "ds_read_b128 a[192:195], v1 offset:8192\n\t" \
  "ds_read_b128 a[196:199], v1 offset:9216\n\t" \
  "ds_read_b128 a[200:203], v1 offset:10240\n\t" \
  "ds_read_b128 a[204:207], v1 offset:11264\n\t" \
  "ds_read_b128 a[208:211], v1 offset:12288\n\t" \
  "ds_read_b128 a[212:215], v1 offset:13312\n\t" \
  "ds_read_b128 a[216:219], v1 offset:14336\n\t" \
  "ds_read_b128 a[220:223], v1 offset:15360\n\t" \
  "ds_read_b128 a[224:227], v1 offset:16384\n\t" \
  "ds_read_b128 a[228:231], v1 offset:17408\n\t" \
  "ds_read_b128 a[232:235], v1 offset:18432\n\t" \
  "ds_read_b128 a[236:239], v1 offset:19456\n\t" \
  "s_waitcnt lgkmcnt(0)\n\t" \
  "s_barrier\n\t" \
  "v_mfma_f32_16x16x32_bf16 v[0:3], v[120:123], a[160:163], 0\n\t" \
  "v_mfma_f32_16x16x32_bf16 v[0:3], v[124:127], a[164:167], v[0:3]\n\t" \
  "ds_read_b128 v[136:139], v152 offset:4096\n\t" \
  "v_mfma_f32_16x16x32_bf16 v[0:3], v[128:131], a[168:171], v[0:3]\n\t" \
  "s_add_u32 %[soff], %[soff], 12288\n\t" \
  "s_add_u32 m0, s72, 16384\n\t" \
  "s_nop 0\n\t" \
  "buffer_load_dwordx4 v172, %[srd], %[soff] offen offset:0 lds\n\t" \
  "v_mfma_f32_16x16x32_bf16 v[0:3], v[132:135], a[172:175], v[0:3]\n\t" \
  "ds_read_b128 v[140:143], v153 offset:4096\n\t" \
  "v_mfma_f32_16x16x32_bf16 v[4:7], v[120:123], a[176:179], 0\n\t" \
  "v_mfma_f32_16x16x32_bf16 v[4:7], v[124:127], a[180:183], v[4:7]\n\t" \
  "ds_read_b128 v[144:147], v154 offset:4096\n\t" \
  "v_mfma_f32_16x16x32_bf16 v[4:7], v[128:131], a[184:187], v[4:7]\n\t" \
  "buffer_load_dwordx4 v172, %[srd], %[soff] offen offset:1024 lds\n\t" \
  "v_mfma_f32_16x16x32_bf16 v[4:7], v[132:135], a[188:191], v[4:7]\n\t" \
  "ds_read_b128 v[148:151], v155 offset:4096\n\t" \
  "v_mfma_f32_16x16x32_bf16 v[8:11], v[120:123], a[192:195], 0\n\t" \
  "v_mfma_f32_16x16x32_bf16 v[8:11], v[124:127], a[196:199], v[8:11]\n\t" \
  "v_mfma_f32_16x16x32_bf16 v[8:11], v[128:131], a[200:203], v[8:11]\n\t" \
  "buffer_load_dwordx4 v172, %[srd], %[soff] offen offset:2048 lds\n\t" \
  "v_mfma_f32_16x16x32_bf16 v[8:11], v[132:135], a[204:207], v[8:11]\n\t" \
  "v_mfma_f32_16x16x32_bf16 v[12:15], v[120:123], a[208:211], 0\n\t" \
  "v_mfma_f32_16x16x32_bf16 v[12:15], v[124:127], a[212:215], v[12:15]\n\t" \
  "v_mfma_f32_16x16x32_bf16 v[12:15], v[128:131], a[216:219], v[12:15]\n\t" \
  "buffer_load_dwordx4 v172, %[srd], %[soff] offen offset:3072 lds\n\t" \
  "v_mfma_f32_16x16x32_bf16 v[12:15], v[132:135], a[220:223], v[12:15]\n\t" \
  "v_mfma_f32_16x16x32_bf16 v[16:19], v[120:123], a[224:227], 0\n\t" \
  "v_mfma_f32_16x16x32_bf16 v[16:19], v[124:127], a[228:231], v[16:19]\n\t" \
  "v_mfma_f32_16x16x32_bf16 v[16:19], v[128:131], a[232:235], v[16:19]\n\t" \
  "s_waitcnt lgkmcnt(0)\n\t" \
  "v_mfma_f32_16x16x32_bf16 v[16:19], v[132:135], a[236:239], v[16:19]\n\t" \
  "v_mfma_f32_16x16x32_bf16 v[20:23], v[136:139], a[160:163], 0\n\t" \
  "v_exp_f32 v0, v0\n\t" \
  "v_mfma_f32_16x16x32_bf16 v[20:23], v[140:143], a[164:167], v[20:23]\n\t" \
  "ds_read_b128 v[120:123], v152 offset:8192\n\t" \
  "v_exp_f32 v1, v1\n\t" \
  "v_mfma_f32_16x16x32_bf16 v[20:23], v[144:147], a[168:171], v[20:23]\n\t" \
  "s_add_u32 %[soff], %[soff], 4096\n\t" \
  "s_add_u32 m0, s72, 20480\n\t" \
  "s_nop 0\n\t" \
  "buffer_load_dwordx4 v172, %[srd], %[soff] offen offset:0 lds\n\t" \
  "v_exp_f32 v2, v2\n\t" \
  "v_mfma_f32_16x16x32_bf16 v[20:23], v[148:151], a[172:175], v[20:23]\n\t" \
  "ds_read_b128 v[124:127], v153 offset:8192\n\t" \
  "v_exp_f32 v3, v3\n\t" \
  "v_mfma_f32_16x16x32_bf16 v[24:27], v[136:139], a[176:179], 0\n\t" \
  "v_exp_f32 v4, v4\n\t" \
  "v_mfma_f32_16x16x32_bf16 v[24:27], v[140:143], a[180:183], v[24:27]\n\t" \
  "ds_read_b128 v[128:131], v154 offset:8192\n\t" \
  "v_exp_f32 v5, v5\n\t" \
  "v_mfma_f32_16x16x32_bf16 v[24:27], v[144:147], a[184:187], v[24:27]\n\t" \
  "buffer_load_dwordx4 v172, %[srd], %[soff] offen offset:1024 lds\n\t" \
  "v_exp_f32 v6, v6\n\t" \
  "v_mfma_f32_16x16x32_bf16 v[24:27], v[148:151], a[188:191], v[24:27]\n\t" \
  "ds_read_b128 v[132:135], v155 offset:8192\n\t" \
  "v_exp_f32 v7, v7\n\t" \
  "v_mfma_f32_16x16x32_bf16 v[28:31], v[136:139], a[192:195], 0\n\t" \
  "v_exp_f32 v8, v8\n\t" \
  "v_mfma_f32_16x16x32_bf16 v[28:31], v[140:143], a[196:199], v[28:31]\n\t" \
  "v_exp_f32 v9, v9\n\t" \
  "v_mfma_f32_16x16x32_bf16 v[28:31], v[144:147], a[200:203], v[28:31]\n\t" \
  "buffer_load_dwordx4 v172, %[srd], %[soff] offen offset:2048 lds\n\t" \
  "v_exp_f32 v10, v10\n\t" \
  "v_mfma_f32_16x16x32_bf16 v[28:31], v[148:151], a[204:207], v[28:31]\n\t" \
  "v_exp_f32 v11, v11\n\t" \
  "v_mfma_f32_16x16x32_bf16 v[32:35], v[136:139], a[208:211], 0\n\t" \
  "v_exp_f32 v12, v12\n\t" \
  "v_mfma_f32_16x16x32_bf16 v[32:35], v[140:143], a[212:215], v[32:35]\n\t" \
  "v_exp_f32 v13, v13\n\t" \
  "v_mfma_f32_16x16x32_bf16 v[32:35], v[144:147], a[216:219], v[32:35]\n\t" \
  "buffer_load_dwordx4 v172, %[srd], %[soff] offen offset:3072 lds\n\t" \
  "v_exp_f32 v14, v14\n\t" \
  "v_mfma_f32_16x16x32_bf16 v[32:35], v[148:151], a[220:223], v[32:35]\n\t" \
  "v_exp_f32 v15, v15\n\t" \
  "v_mfma_f32_16x16x32_bf16 v[36:39], v[136:139], a[224:227], 0\n\t" \
  "v_exp_f32 v16, v16\n\t" \
  "v_mfma_f32_16x16x32_bf16 v[36:39], v[140:143], a[228:231], v[36:39]\n\t" \
  "v_exp_f32 v17, v17\n\t" \
  "v_mfma_f32_16x16x32_bf16 v[36:39], v[144:147], a[232:235], v[36:39]\n\t" \
  "s_waitcnt lgkmcnt(0)\n\t" \
  "v_exp_f32 v18, v18\n\t" \
  "v_mfma_f32_16x16x32_bf16 v[36:39], v[148:151], a[236:239], v[36:39]\n\t" \
  "v_exp_f32 v19, v19\n\t" \
  "s_nop 0\n\t" \
  "v_cvt_pk_bf16_f32 v40, v0, v1\n\t" \
  "v_cvt_pk_bf16_f32 v41, v2, v3\n\t" \
  "v_cvt_pk_bf16_f32 v44, v4, v5\n\t" \
  "v_cvt_pk_bf16_f32 v45, v6, v7\n\t" \
  "v_cvt_pk_bf16_f32 v48, v8, v9\n\t" \
  "v_cvt_pk_bf16_f32 v49, v10, v11\n\t" \
  "v_cvt_pk_bf16_f32 v52, v12, v13\n\t" \
  "v_cvt_pk_bf16_f32 v53, v14, v15\n\t" \
  "v_cvt_pk_bf16_f32 v56, v16, v17\n\t" \
  "v_cvt_pk_bf16_f32 v57, v18, v19\n\t" \
  "v_mfma_f32_16x16x32_bf16 v[0:3], v[120:123], a[160:163], 0\n\t" \
  "v_exp_f32 v20, v20\n\t" \
  "v_mfma_f32_16x16x32_bf16 v[0:3], v[124:127], a[164:167], v[0:3]\n\t" \
  "ds_read_b128 v[136:139], v152 offset:12288\n\t" \
  "v_exp_f32 v21, v21\n\t" \
  "v_mfma_f32_16x16x32_bf16 v[0:3], v[128:131], a[168:171], v[0:3]\n\t" \
  "s_add_u32 %[soff], %[soff], 12288\n\t" \
  "s_add_u32 m0, s72, 32768\n\t" \
  "s_nop 0\n\t" \
  "buffer_load_dwordx4 v172, %[srd], %[soff] offen offset:0 lds\n\t" \
  "v_exp_f32 v22, v22\n\t" \
  "v_mfma_f32_16x16x32_bf16 v[0:3], v[132:135], a[172:175], v[0:3]\n\t" \
  "ds_read_b128 v[140:143], v153 offset:12288\n\t" \
  "v_exp_f32 v23, v23\n\t" \
  "v_mfma_f32_16x16x32_bf16 v[4:7], v[120:123], a[176:179], 0\n\t" \
  "v_exp_f32 v24, v24\n\t" \
  "v_mfma_f32_16x16x32_bf16 v[4:7], v[124:127], a[180:183], v[4:7]\n\t" \
  "ds_read_b128 v[144:147], v154 offset:12288\n\t" \
  "v_exp_f32 v25, v25\n\t" \
  "v_mfma_f32_16x16x32_bf16 v[4:7], v[128:131], a[184:187], v[4:7]\n\t" \
  "buffer_load_dwordx4 v172, %[srd], %[soff] offen offset:1024 lds\n\t" \
  "v_exp_f32 v26, v26\n\t" \
  "v_mfma_f32_16x16x32_bf16 v[4:7], v[132:135], a[188:191], v[4:7]\n\t" \
  "ds_read_b128 v[148:151], v155 offset:12288\n\t" \
  "v_exp_f32 v27, v27\n\t" \
  "v_mfma_f32_16x16x32_bf16 v[8:11], v[120:123], a[192:195], 0\n\t" \
  "v_exp_f32 v28, v28\n\t" \
  "v_mfma_f32_16x16x32_bf16 v[8:11], v[124:127], a[196:199], v[8:11]\n\t" \
  "v_exp_f32 v29, v29\n\t" \
  "v_mfma_f32_16x16x32_bf16 v[8:11], v[128:131], a[200:203], v[8:11]\n\t" \
  "buffer_load_dwordx4 v172, %[srd], %[soff] offen offset:2048 lds\n\t" \
  "v_exp_f32 v30, v30\n\t" \
  "v_mfma_f32_16x16x32_bf16 v[8:11], v[132:135], a[204:207], v[8:11]\n\t" \
  "v_exp_f32 v31, v31\n\t" \
  "v_mfma_f32_16x16x32_bf16 v[12:15], v[120:123], a[208:211], 0\n\t" \
  "v_exp_f32 v32, v32\n\t" \
  "v_mfma_f32_16x16x32_bf16 v[12:15], v[124:127], a[212:215], v[12:15]\n\t" \
  "v_exp_f32 v33, v33\n\t" \
  "v_mfma_f32_16x16x32_bf16 v[12:15], v[128:131], a[216:219], v[12:15]\n\t" \
  "buffer_load_dwordx4 v172, %[srd], %[soff] offen offset:3072 lds\n\t" \
  "v_exp_f32 v34, v34\n\t" \
  "v_mfma_f32_16x16x32_bf16 v[12:15], v[132:135], a[220:223], v[12:15]\n\t" \
  "v_exp_f32 v35, v35\n\t" \
  "v_mfma_f32_16x16x32_bf16 v[16:19], v[120:123], a[224:227], 0\n\t" \
  "v_exp_f32 v36, v36\n\t" \
  "v_mfma_f32_16x16x32_bf16 v[16:19], v[124:127], a[228:231], v[16:19]\n\t" \
  "v_exp_f32 v37, v37\n\t" \
  "v_mfma_f32_16x16x32_bf16 v[16:19], v[128:131], a[232:235], v[16:19]\n\t" \
  "s_waitcnt lgkmcnt(0)\n\t" \
  "v_exp_f32 v38, v38\n\t" \
  "v_mfma_f32_16x16x32_bf16 v[16:19], v[132:135], a[236:239], v[16:19]\n\t" \
  "v_exp_f32 v39, v39\n\t" \
  "s_nop 0\n\t" \
  "v_cvt_pk_bf16_f32 v42, v20, v21\n\t" \
  "v_cvt_pk_bf16_f32 v43, v22, v23\n\t" \
  "v_cvt_pk_bf16_f32 v46, v24, v25\n\t" \
  "v_cvt_pk_bf16_f32 v47, v26, v27\n\t" \
  "v_cvt_pk_bf16_f32 v50, v28, v29\n\t" \
  "v_cvt_pk_bf16_f32 v51, v30, v31\n\t" \
  "v_cvt_pk_bf16_f32 v54, v32, v33\n\t" \
  "v_cvt_pk_bf16_f32 v55, v34, v35\n\t" \
  "v_cvt_pk_bf16_f32 v58, v36, v37\n\t" \
  "v_cvt_pk_bf16_f32 v59, v38, v39\n\t" \
  "v_mfma_f32_16x16x32_bf16 v[20:23], v[136:139], a[160:163], 0\n\t" \
  "s_waitcnt vmcnt(4)\n\t" \
  "s_barrier\n\t" \
  "v_exp_f32 v0, v0\n\t" \
  "v_mfma_f32_16x16x32_bf16 v[20:23], v[140:143], a[164:167], v[20:23]\n\t" \
  "ds_read_b128 v[120:123], v152 offset:16384\n\t" \
  "v_exp_f32 v1, v1\n\t" \
  "v_mfma_f32_16x16x32_bf16 v[20:23], v[144:147], a[168:171], v[20:23]\n\t" \
  "s_add_u32 %[soff], %[soff], 4096\n\t" \
  "s_add_u32 m0, s72, 36864\n\t" \
  "s_nop 0\n\t" \
  "buffer_load_dwordx4 v172, %[srd], %[soff] offen offset:0 lds\n\t" \
  "v_exp_f32 v2, v2\n\t" \
  "v_mfma_f32_16x16x32_bf16 v[20:23], v[148:151], a[172:175], v[20:23]\n\t" \
  "ds_read_b128 v[124:127], v153 offset:16384\n\t" \
  "v_exp_f32 v3, v3\n\t" \
  "v_mfma_f32_16x16x32_bf16 v[24:27], v[136:139], a[176:179], 0\n\t" \
  "v_exp_f32 v4, v4\n\t" \
  "v_mfma_f32_16x16x32_bf16 v[24:27], v[140:143], a[180:183], v[24:27]\n\t" \
  "ds_read_b128 v[128:131], v154 offset:16384\n\t" \
  "v_exp_f32 v5, v5\n\t" \
  "v_mfma_f32_16x16x32_bf16 v[24:27], v[144:147], a[184:187], v[24:27]\n\t" \
  "buffer_load_dwordx4 v172, %[srd], %[soff] offen offset:1024 lds\n\t" \
  "v_exp_f32 v6, v6\n\t" \
  "v_mfma_f32_16x16x32_bf16 v[24:27], v[148:151], a[188:191], v[24:27]\n\t" \
  "ds_read_b128 v[132:135], v155 offset:16384\n\t" \
  "v_exp_f32 v7, v7\n\t" \
  "v_mfma_f32_16x16x32_bf16 v[28:31], v[136:139], a[192:195], 0\n\t" \
  "v_exp_f32 v8, v8\n\t" \
  "v_mfma_f32_16x16x32_bf16 v[28:31], v[140:143], a[196:199], v[28:31]\n\t" \
  "v_exp_f32 v9, v9\n\t" \
  "v_mfma_f32_16x16x32_bf16 v[28:31], v[144:147], a[200:203], v[28:31]\n\t" \
  "buffer_load_dwordx4 v172, %[srd], %[soff] offen offset:2048 lds\n\t" \
  "v_exp_f32 v10, v10\n\t" \
  "v_mfma_f32_16x16x32_bf16 v[28:31], v[148:151], a[204:207], v[28:31]\n\t" \
  "v_exp_f32 v11, v11\n\t" \
  "v_mfma_f32_16x16x32_bf16 v[32:35], v[136:139], a[208:211], 0\n\t" \
  "v_exp_f32 v12, v12\n\t" \
  "v_mfma_f32_16x16x32_bf16 v[32:35], v[140:143], a[212:215], v[32:35]\n\t" \
  "v_exp_f32 v13, v13\n\t" \
  "v_mfma_f32_16x16x32_bf16 v[32:35], v[144:147], a[216:219], v[32:35]\n\t" \
  "buffer_load_dwordx4 v172, %[srd], %[soff] offen offset:3072 lds\n\t" \
  "v_exp_f32 v14, v14\n\t" \
  "v_mfma_f32_16x16x32_bf16 v[32:35], v[148:151], a[220:223], v[32:35]\n\t" \
  "v_exp_f32 v15, v15\n\t" \
  "v_mfma_f32_16x16x32_bf16 v[36:39], v[136:139], a[224:227], 0\n\t" \
  "v_exp_f32 v16, v16\n\t" \
  "v_mfma_f32_16x16x32_bf16 v[36:39], v[140:143], a[228:231], v[36:39]\n\t" \
  "v_exp_f32 v17, v17\n\t" \
  "v_mfma_f32_16x16x32_bf16 v[36:39], v[144:147], a[232:235], v[36:39]\n\t" \
  "s_waitcnt lgkmcnt(0)\n\t" \
  "v_exp_f32 v18, v18\n\t" \
  "v_mfma_f32_16x16x32_bf16 v[36:39], v[148:151], a[236:239], v[36:39]\n\t" \
  "v_exp_f32 v19, v19\n\t" \
  "s_nop 0\n\t" \
  "v_cvt_pk_bf16_f32 v60, v0, v1\n\t" \
  "v_cvt_pk_bf16_f32 v61, v2, v3\n\t" \
  "v_cvt_pk_bf16_f32 v64, v4, v5\n\t" \
  "v_cvt_pk_bf16_f32 v65, v6, v7\n\t" \
  "v_cvt_pk_bf16_f32 v68, v8, v9\n\t" \
  "v_cvt_pk_bf16_f32 v69, v10, v11\n\t" \
  "v_cvt_pk_bf16_f32 v72, v12, v13\n\t" \
  "v_cvt_pk_bf16_f32 v73, v14, v15\n\t" \
  "v_cvt_pk_bf16_f32 v76, v16, v17\n\t" \
  "v_cvt_pk_bf16_f32 v77, v18, v19\n\t" \
  "s_nop 7\n\t" \
  "v_exp_f32 v20, v20\n\t" \
  "v_exp_f32 v21, v21\n\t" \
  "v_exp_f32 v22, v22\n\t" \
  "v_exp_f32 v23, v23\n\t" \
  "v_exp_f32 v24, v24\n\t" \
  "v_exp_f32 v25, v25\n\t" \
  "v_exp_f32 v26, v26\n\t" \
  "v_exp_f32 v27, v27\n\t" \
  "v_exp_f32 v28, v28\n\t" \
  "v_exp_f32 v29, v29\n\t" \
  "s_nop 0\n\t" \
  "v_cvt_pk_bf16_f32 v62, v20, v21\n\t" \
  "v_cvt_pk_bf16_f32 v63, v22, v23\n\t" \
  "v_cvt_pk_bf16_f32 v66, v24, v25\n\t" \
  "v_cvt_pk_bf16_f32 v67, v26, v27\n\t" \
  "v_cvt_pk_bf16_f32 v70, v28, v29\n\t" \
  "s_nop 1\n\t" \
  "v_mfma_f32_16x16x32_bf16 v[0:3], v[120:123], a[160:163], 0\n\t" \
  "v_exp_f32 v30, v30\n\t" \
  "v_mfma_f32_16x16x32_bf16 v[0:3], v[124:127], a[164:167], v[0:3]\n\t" \
  "ds_read_b64_tr_b16 v[136:137], v156 offset:0\n\t" \
  "v_mfma_f32_16x16x32_bf16 v[0:3], v[128:131], a[168:171], v[0:3]\n\t" \
  "v_exp_f32 v31, v31\n\t" \
  "v_mfma_f32_16x16x32_bf16 v[0:3], v[132:135], a[172:175], v[0:3]\n\t" \
  "ds_read_b64_tr_b16 v[138:139], v156 offset:4096\n\t" \
  "v_cvt_pk_bf16_f32 v71, v30, v31\n\t" \
  "v_mfma_f32_16x16x32_bf16 v[4:7], v[120:123], a[176:179], 0\n\t" \
  "v_exp_f32 v32, v32\n\t" \
  "v_mfma_f32_16x16x32_bf16 v[4:7], v[124:127], a[180:183], v[4:7]\n\t" \
  "ds_read_b64_tr_b16 v[140:141], v156 offset:8192\n\t" \
  "v_mfma_f32_16x16x32_bf16 v[4:7], v[128:131], a[184:187], v[4:7]\n\t" \
  "v_exp_f32 v33, v33\n\t" \
  "v_mfma_f32_16x16x32_bf16 v[4:7], v[132:135], a[188:191], v[4:7]\n\t" \
  "ds_read_b64_tr_b16 v[142:143], v156 offset:12288\n\t" \
  "v_cvt_pk_bf16_f32 v74, v32, v33\n\t" \
  "v_mfma_f32_16x16x32_bf16 v[8:11], v[120:123], a[192:195], 0\n\t" \
  "v_exp_f32 v34, v34\n\t" \
  "v_mfma_f32_16x16x32_bf16 v[8:11], v[124:127], a[196:199], v[8:11]\n\t" \
  "ds_read_b64_tr_b16 v[144:145], v157 offset:0\n\t" \
  "v_mfma_f32_16x16x32_bf16 v[8:11], v[128:131], a[200:203], v[8:11]\n\t" \
  "v_exp_f32 v35, v35\n\t" \
  "v_mfma_f32_16x16x32_bf16 v[8:11], v[132:135], a[204:207], v[8:11]\n\t" \
  "ds_read_b64_tr_b16 v[146:147], v157 offset:4096\n\t" \
  "v_cvt_pk_bf16_f32 v75, v34, v35\n\t" \
  "v_mfma_f32_16x16x32_bf16 v[12:15], v[120:123], a[208:211], 0\n\t" \
  "v_exp_f32 v36, v36\n\t" \
  "v_mfma_f32_16x16x32_bf16 v[12:15], v[124:127], a[212:215], v[12:15]\n\t" \
  "ds_read_b64_tr_b16 v[148:149], v157 offset:8192\n\t" \
  "v_mfma_f32_16x16x32_bf16 v[12:15], v[128:131], a[216:219], v[12:15]\n\t" \
  "v_exp_f32 v37, v37\n\t" \
  "v_mfma_f32_16x16x32_bf16 v[12:15], v[132:135], a[220:223], v[12:15]\n\t" \
  "ds_read_b64_tr_b16 v[150:151], v157 offset:12288\n\t" \
  "v_cvt_pk_bf16_f32 v78, v36, v37\n\t" \
  "v_mfma_f32_16x16x32_bf16 v[16:19], v[120:123], a[224:227], 0\n\t" \
  "v_exp_f32 v38, v38\n\t" \
  "v_mfma_f32_16x16x32_bf16 v[16:19], v[124:127], a[228:231], v[16:19]\n\t" \
  "v_mfma_f32_16x16x32_bf16 v[16:19], v[128:131], a[232:235], v[16:19]\n\t" \
  "s_waitcnt lgkmcnt(4)\n\t" \
  "v_exp_f32 v39, v39\n\t" \
  "v_mfma_f32_16x16x32_bf16 v[16:19], v[132:135], a[236:239], v[16:19]\n\t" \
  "v_cvt_pk_bf16_f32 v79, v38, v39\n\t" \
  "v_mfma_f32_16x16x32_bf16 a[0:3], v[136:139], v[40:43], 0\n\t" \
  "v_exp_f32 v0, v0\n\t" \
  "v_mfma_f32_16x16x32_bf16 a[0:3], v[140:143], v[60:63], a[0:3]\n\t" \
  "ds_read_b128 v[120:123], v152 offset:20480\n\t" \
  "v_mfma_f32_16x16x32_bf16 a[4:7], v[136:139], v[44:47], 0\n\t" \
  "v_exp_f32 v1, v1\n\t" \
  "v_mfma_f32_16x16x32_bf16 a[4:7], v[140:143], v[64:67], a[4:7]\n\t" \
  "ds_read_b128 v[124:127], v153 offset:20480\n\t" \
  "v_cvt_pk_bf16_f32 v80, v0, v1\n\t" \
  "v_mfma_f32_16x16x32_bf16 a[8:11], v[136:139], v[48:51], 0\n\t" \
  "s_waitcnt lgkmcnt(2)\n\t" \
  "v_exp_f32 v2, v2\n\t" \
  "v_mfma_f32_16x16x32_bf16 a[8:11], v[140:143], v[68:71], a[8:11]\n\t" \
  "ds_read_b128 v[128:131], v154 offset:20480\n\t" \
  "v_mfma_f32_16x16x32_bf16 a[12:15], v[136:139], v[52:55], 0\n\t" \
  "v_exp_f32 v3, v3\n\t" \
  "v_mfma_f32_16x16x32_bf16 a[12:15], v[140:143], v[72:75], a[12:15]\n\t" \
  "ds_read_b128 v[132:135], v155 offset:20480\n\t" \
  "v_cvt_pk_bf16_f32 v81, v2, v3\n\t" \
  "v_mfma_f32_16x16x32_bf16 a[16:19], v[136:139], v[56:59], 0\n\t" \
  "v_exp_f32 v4, v4\n\t" \
  "v_mfma_f32_16x16x32_bf16 a[16:19], v[140:143], v[76:79], a[16:19]\n\t" \
  "v_mfma_f32_16x16x32_bf16 a[20:23], v[144:147], v[40:43], 0\n\t" \
  "v_exp_f32 v5, v5\n\t" \
  "v_mfma_f32_16x16x32_bf16 a[20:23], v[148:151], v[60:63], a[20:23]\n\t" \
  "v_cvt_pk_bf16_f32 v84, v4, v5\n\t" \
  "v_mfma_f32_16x16x32_bf16 a[24:27], v[144:147], v[44:47], 0\n\t" \
  "v_exp_f32 v6, v6\n\t" \
  "v_mfma_f32_16x16x32_bf16 a[24:27], v[148:151], v[64:67], a[24:27]\n\t" \
  "v_mfma_f32_16x16x32_bf16 a[28:31], v[144:147], v[48:51], 0\n\t" \
  "v_exp_f32 v7, v7\n\t" \
  "v_mfma_f32_16x16x32_bf16 a[28:31], v[148:151], v[68:71], a[28:31]\n\t" \
  "v_cvt_pk_bf16_f32 v85, v6, v7\n\t" \
  "v_mfma_f32_16x16x32_bf16 a[32:35], v[144:147], v[52:55], 0\n\t" \
  "v_exp_f32 v8, v8\n\t" \
  "v_mfma_f32_16x16x32_bf16 a[32:35], v[148:151], v[72:75], a[32:35]\n\t" \
  "v_mfma_f32_16x16x32_bf16 a[36:39], v[144:147], v[56:59], 0\n\t" \
  "s_waitcnt lgkmcnt(0)\n\t" \
  "v_exp_f32 v9, v9\n\t" \
  "v_mfma_f32_16x16x32_bf16 a[36:39], v[148:151], v[76:79], a[36:39]\n\t" \
  "v_cvt_pk_bf16_f32 v88, v8, v9\n\t" \
  "v_mfma_f32_16x16x32_bf16 v[20:23], v[120:123], a[160:163], 0\n\t" \
  "v_exp_f32 v10, v10\n\t" \
  "v_mfma_f32_16x16x32_bf16 v[20:23], v[124:127], a[164:167], v[20:23]\n\t" \
  "ds_read_b64_tr_b16 v[136:137], v158 offset:0\n\t" \
  "v_mfma_f32_16x16x32_bf16 v[20:23], v[128:131], a[168:171], v[20:23]\n\t" \
  "v_exp_f32 v11, v11\n\t" \
  "v_mfma_f32_16x16x32_bf16 v[20:23], v[132:135], a[172:175], v[20:23]\n\t" \
  "ds_read_b64_tr_b16 v[138:139], v158 offset:4096\n\t" \
  "v_cvt_pk_bf16_f32 v89, v10, v11\n\t" \
  "v_mfma_f32_16x16x32_bf16 v[24:27], v[120:123], a[176:179], 0\n\t" \
  "v_exp_f32 v12, v12\n\t" \
  "v_mfma_f32_16x16x32_bf16 v[24:27], v[124:127], a[180:183], v[24:27]\n\t" \
  "ds_read_b64_tr_b16 v[140:141], v158 offset:8192\n\t" \
  "v_mfma_f32_16x16x32_bf16 v[24:27], v[128:131], a[184:187], v[24:27]\n\t" \
  "v_exp_f32 v13, v13\n\t" \
  "v_mfma_f32_16x16x32_bf16 v[24:27], v[132:135], a[188:191], v[24:27]\n\t" \
  "ds_read_b64_tr_b16 v[142:143], v158 offset:12288\n\t" \
  "v_cvt_pk_bf16_f32 v92, v12, v13\n\t" \
  "v_mfma_f32_16x16x32_bf16 v[28:31], v[120:123], a[192:195], 0\n\t" \
  "v_exp_f32 v14, v14\n\t" \
  "v_mfma_f32_16x16x32_bf16 v[28:31], v[124:127], a[196:199], v[28:31]\n\t" \
  "ds_read_b64_tr_b16 v[144:145], v159 offset:0\n\t" \
  "v_mfma_f32_16x16x32_bf16 v[28:31], v[128:131], a[200:203], v[28:31]\n\t" \
  "v_exp_f32 v15, v15\n\t" \
  "v_mfma_f32_16x16x32_bf16 v[28:31], v[132:135], a[204:207], v[28:31]\n\t" \
  "ds_read_b64_tr_b16 v[146:147], v159 offset:4096\n\t" \
  "v_cvt_pk_bf16_f32 v93, v14, v15\n\t" \
  "v_mfma_f32_16x16x32_bf16 v[32:35], v[120:123], a[208:211], 0\n\t" \
  "v_exp_f32 v16, v16\n\t" \
  "v_mfma_f32_16x16x32_bf16 v[32:35], v[124:127], a[212:215], v[32:35]\n\t" \
  "ds_read_b64_tr_b16 v[148:149], v159 offset:8192\n\t" \
  "v_mfma_f32_16x16x32_bf16 v[32:35], v[128:131], a[216:219], v[32:35]\n\t" \
  "v_exp_f32 v17, v17\n\t" \
  "v_mfma_f32_16x16x32_bf16 v[32:35], v[132:135], a[220:223], v[32:35]\n\t" \
  "ds_read_b64_tr_b16 v[150:151], v159 offset:12288\n\t" \
  "v_cvt_pk_bf16_f32 v96, v16, v17\n\t" \
  "v_mfma_f32_16x16x32_bf16 v[36:39], v[120:123], a[224:227], 0\n\t" \
  "v_exp_f32 v18, v18\n\t" \
  "v_mfma_f32_16x16x32_bf16 v[36:39], v[124:127], a[228:231], v[36:39]\n\t" \
  "v_mfma_f32_16x16x32_bf16 v[36:39], v[128:131], a[232:235], v[36:39]\n\t" \
  "s_waitcnt lgkmcnt(4)\n\t" \
  "v_exp_f32 v19, v19\n\t" \
  "v_mfma_f32_16x16x32_bf16 v[36:39], v[132:135], a[236:239], v[36:39]\n\t" \
  "v_cvt_pk_bf16_f32 v97, v18, v19\n\t" \
  "v_mfma_f32_16x16x32_bf16 a[40:43], v[136:139], v[40:43], 0\n\t" \
  "v_exp_f32 v20, v20\n\t" \
  "v_mfma_f32_16x16x32_bf16 a[40:43], v[140:143], v[60:63], a[40:43]\n\t" \
  "ds_read_b128 v[120:123], v152 offset:24576\n\t" \
  "v_mfma_f32_16x16x32_bf16 a[44:47], v[136:139], v[44:47], 0\n\t" \
  "v_exp_f32 v21, v21\n\t" \
  "v_mfma_f32_16x16x32_bf16 a[44:47], v[140:143], v[64:67], a[44:47]\n\t" \
  "ds_read_b128 v[124:127], v153 offset:24576\n\t" \
  "v_cvt_pk_bf16_f32 v82, v20, v21\n\t" \
  "v_mfma_f32_16x16x32_bf16 a[48:51], v[136:139], v[48:51], 0\n\t" \
  "s_waitcnt lgkmcnt(2)\n\t" \
  "v_exp_f32 v22, v22\n\t" \
  "v_mfma_f32_16x16x32_bf16 a[48:51], v[140:143], v[68:71], a[48:51]\n\t" \
  "ds_read_b128 v[128:131], v154 offset:24576\n\t" \
  "v_mfma_f32_16x16x32_bf16 a[52:55], v[136:139], v[52:55], 0\n\t" \
  "v_exp_f32 v23, v23\n\t" \
  "v_mfma_f32_16x16x32_bf16 a[52:55], v[140:143], v[72:75], a[52:55]\n\t" \
  "ds_read_b128 v[132:135], v155 offset:24576\n\t" \
  "v_cvt_pk_bf16_f32 v83, v22, v23\n\t" \
  "v_mfma_f32_16x16x32_bf16 a[56:59], v[136:139], v[56:59], 0\n\t" \
  "v_exp_f32 v24, v24\n\t" \
  "v_mfma_f32_16x16x32_bf16 a[56:59], v[140:143], v[76:79], a[56:59]\n\t" \
  "v_mfma_f32_16x16x32_bf16 a[60:63], v[144:147], v[40:43], 0\n\t" \
  "v_exp_f32 v25, v25\n\t" \
  "v_mfma_f32_16x16x32_bf16 a[60:63], v[148:151], v[60:63], a[60:63]\n\t" \
  "v_cvt_pk_bf16_f32 v86, v24, v25\n\t" \
  "v_mfma_f32_16x16x32_bf16 a[64:67], v[144:147], v[44:47], 0\n\t" \
  "v_exp_f32 v26, v26\n\t" \
  "v_mfma_f32_16x16x32_bf16 a[64:67], v[148:151], v[64:67], a[64:67]\n\t" \
  "v_mfma_f32_16x16x32_bf16 a[68:71], v[144:147], v[48:51], 0\n\t" \
  "v_exp_f32 v27, v27\n\t" \
  "v_mfma_f32_16x16x32_bf16 a[68:71], v[148:151], v[68:71], a[68:71]\n\t" \
  "v_cvt_pk_bf16_f32 v87, v26, v27\n\t" \
  "v_mfma_f32_16x16x32_bf16 a[72:75], v[144:147], v[52:55], 0\n\t" \
  "v_exp_f32 v28, v28\n\t" \
  "v_mfma_f32_16x16x32_bf16 a[72:75], v[148:151], v[72:75], a[72:75]\n\t" \
  "v_mfma_f32_16x16x32_bf16 a[76:79], v[144:147], v[56:59], 0\n\t" \
  "s_waitcnt lgkmcnt(0)\n\t" \
  "v_exp_f32 v29, v29\n\t" \
  "v_mfma_f32_16x16x32_bf16 a[76:79], v[148:151], v[76:79], a[76:79]\n\t" \
  "v_cvt_pk_bf16_f32 v90, v28, v29\n\t" \
  "v_mfma_f32_16x16x32_bf16 v[0:3], v[120:123], a[160:163], 0\n\t" \
  "v_exp_f32 v30, v30\n\t" \
  "v_mfma_f32_16x16x32_bf16 v[0:3], v[124:127], a[164:167], v[0:3]\n\t" \
  "ds_read_b64_tr_b16 v[136:137], v160 offset:0\n\t" \
  "v_mfma_f32_16x16x32_bf16 v[0:3], v[128:131], a[168:171], v[0:3]\n\t" \
  "v_exp_f32 v31, v31\n\t" \
  "v_mfma_f32_16x16x32_bf16 v[0:3], v[132:135], a[172:175], v[0:3]\n\t" \
  "ds_read_b64_tr_b16 v[138:139], v160 offset:4096\n\t" \
  "v_cvt_pk_bf16_f32 v91, v30, v31\n\t" \
  "v_mfma_f32_16x16x32_bf16 v[4:7], v[120:123], a[176:179], 0\n\t" \
  "v_exp_f32 v32, v32\n\t" \
  "v_mfma_f32_16x16x32_bf16 v[4:7], v[124:127], a[180:183], v[4:7]\n\t" \
  "ds_read_b64_tr_b16 v[140:141], v160 offset:8192\n\t" \
  "v_mfma_f32_16x16x32_bf16 v[4:7], v[128:131], a[184:187], v[4:7]\n\t" \
  "v_exp_f32 v33, v33\n\t" \
  "v_mfma_f32_16x16x32_bf16 v[4:7], v[132:135], a[188:191], v[4:7]\n\t" \
  "ds_read_b64_tr_b16 v[142:143], v160 offset:12288\n\t" \
  "v_cvt_pk_bf16_f32 v94, v32, v33\n\t" \
  "v_mfma_f32_16x16x32_bf16 v[8:11], v[120:123], a[192:195], 0\n\t" \
  "v_exp_f32 v34, v34\n\t" \
  "v_mfma_f32_16x16x32_bf16 v[8:11], v[124:127], a[196:199], v[8:11]\n\t" \
  "ds_read_b64_tr_b16 v[144:145], v161 offset:0\n\t" \
  "v_mfma_f32_16x16x32_bf16 v[8:11], v[128:131], a[200:203], v[8:11]\n\t" \
  "v_exp_f32 v35, v35\n\t" \
  "v_mfma_f32_16x16x32_bf16 v[8:11], v[132:135], a[204:207], v[8:11]\n\t" \
  "ds_read_b64_tr_b16 v[146:147], v161 offset:4096\n\t" \
  "v_cvt_pk_bf16_f32 v95, v34, v35\n\t" \
  "v_mfma_f32_16x16x32_bf16 v[12:15], v[120:123], a[208:211], 0\n\t" \
  "v_exp_f32 v36, v36\n\t" \
  "v_mfma_f32_16x16x32_bf16 v[12:15], v[124:127], a[212:215], v[12:15]\n\t" \
  "ds_read_b64_tr_b16 v[148:149], v161 offset:8192\n\t" \
  "v_mfma_f32_16x16x32_bf16 v[12:15], v[128:131], a[216:219], v[12:15]\n\t" \
  "v_exp_f32 v37, v37\n\t" \
  "v_mfma_f32_16x16x32_bf16 v[12:15], v[132:135], a[220:223], v[12:15]\n\t" \
  "ds_read_b64_tr_b16 v[150:151], v161 offset:12288\n\t" \
  "v_cvt_pk_bf16_f32 v98, v36, v37\n\t" \
  "v_mfma_f32_16x16x32_bf16 v[16:19], v[120:123], a[224:227], 0\n\t" \
  "v_exp_f32 v38, v38\n\t" \
  "v_mfma_f32_16x16x32_bf16 v[16:19], v[124:127], a[228:231], v[16:19]\n\t" \
  "v_mfma_f32_16x16x32_bf16 v[16:19], v[128:131], a[232:235], v[16:19]\n\t" \
  "s_waitcnt lgkmcnt(4)\n\t" \
  "v_exp_f32 v39, v39\n\t" \
  "v_mfma_f32_16x16x32_bf16 v[16:19], v[132:135], a[236:239], v[16:19]\n\t" \
  "v_cvt_pk_bf16_f32 v99, v38, v39\n\t" \
  "v_mfma_f32_16x16x32_bf16 a[80:83], v[136:139], v[40:43], 0\n\t" \
  "v_exp_f32 v0, v0\n\t" \
  "v_mfma_f32_16x16x32_bf16 a[80:83], v[140:143], v[60:63], a[80:83]\n\t" \
  "ds_read_b128 v[120:123], v152 offset:28672\n\t" \
  "v_mfma_f32_16x16x32_bf16 a[84:87], v[136:139], v[44:47], 0\n\t" \
  "v_exp_f32 v1, v1\n\t" \
  "v_mfma_f32_16x16x32_bf16 a[84:87], v[140:143], v[64:67], a[84:87]\n\t" \
  "ds_read_b128 v[124:127], v153 offset:28672\n\t" \
  "v_cvt_pk_bf16_f32 v100, v0, v1\n\t" \
  "v_mfma_f32_16x16x32_bf16 a[88:91], v[136:139], v[48:51], 0\n\t" \
  "s_waitcnt lgkmcnt(2)\n\t" \
  "v_exp_f32 v2, v2\n\t" \
  "v_mfma_f32_16x16x32_bf16 a[88:91], v[140:143], v[68:71], a[88:91]\n\t" \
  "ds_read_b128 v[128:131], v154 offset:28672\n\t" \
  "v_mfma_f32_16x16x32_bf16 a[92:95], v[136:139], v[52:55], 0\n\t" \
  "v_exp_f32 v3, v3\n\t" \
  "v_mfma_f32_16x16x32_bf16 a[92:95], v[140:143], v[72:75], a[92:95]\n\t" \
  "ds_read_b128 v[132:135], v155 offset:28672\n\t" \
  "v_cvt_pk_bf16_f32 v101, v2, v3\n\t" \
  "v_mfma_f32_16x16x32_bf16 a[96:99], v[136:139], v[56:59], 0\n\t" \
  "v_exp_f32 v4, v4\n\t" \
  "v_mfma_f32_16x16x32_bf16 a[96:99], v[140:143], v[76:79], a[96:99]\n\t" \
  "v_mfma_f32_16x16x32_bf16 a[100:103], v[144:147], v[40:43], 0\n\t" \
  "v_exp_f32 v5, v5\n\t" \
  "v_mfma_f32_16x16x32_bf16 a[100:103], v[148:151], v[60:63], a[100:103]\n\t" \
  "v_cvt_pk_bf16_f32 v104, v4, v5\n\t" \
  "v_mfma_f32_16x16x32_bf16 a[104:107], v[144:147], v[44:47], 0\n\t" \
  "v_exp_f32 v6, v6\n\t" \
  "v_mfma_f32_16x16x32_bf16 a[104:107], v[148:151], v[64:67], a[104:107]\n\t" \
  "v_mfma_f32_16x16x32_bf16 a[108:111], v[144:147], v[48:51], 0\n\t" \
  "v_exp_f32 v7, v7\n\t" \
  "v_mfma_f32_16x16x32_bf16 a[108:111], v[148:151], v[68:71], a[108:111]\n\t" \
  "v_cvt_pk_bf16_f32 v105, v6, v7\n\t" \
  "v_mfma_f32_16x16x32_bf16 a[112:115], v[144:147], v[52:55], 0\n\t" \
  "v_exp_f32 v8, v8\n\t" \
  "v_mfma_f32_16x16x32_bf16 a[112:115], v[148:151], v[72:75], a[112:115]\n\t" \
  "v_mfma_f32_16x16x32_bf16 a[116:119], v[144:147], v[56:59], 0\n\t" \
  "s_waitcnt lgkmcnt(0)\n\t" \
  "v_exp_f32 v9, v9\n\t" \
  "v_mfma_f32_16x16x32_bf16 a[116:119], v[148:151], v[76:79], a[116:119]\n\t" \
  "v_cvt_pk_bf16_f32 v108, v8, v9\n\t" \
  "v_mfma_f32_16x16x32_bf16 v[20:23], v[120:123], a[160:163], 0\n\t" \
  "v_exp_f32 v10, v10\n\t" \
  "v_mfma_f32_16x16x32_bf16 v[20:23], v[124:127], a[164:167], v[20:23]\n\t" \
  "ds_read_b64_tr_b16 v[136:137], v162 offset:0\n\t" \
  "v_mfma_f32_16x16x32_bf16 v[20:23], v[128:131], a[168:171], v[20:23]\n\t" \
  "v_exp_f32 v11, v11\n\t" \
  "v_mfma_f32_16x16x32_bf16 v[20:23], v[132:135], a[172:175], v[20:23]\n\t" \
  "ds_read_b64_tr_b16 v[138:139], v162 offset:4096\n\t" \
  "v_cvt_pk_bf16_f32 v109, v10, v11\n\t" \
  "v_mfma_f32_16x16x32_bf16 v[24:27], v[120:123], a[176:179], 0\n\t" \
  "v_exp_f32 v12, v12\n\t" \
  "v_mfma_f32_16x16x32_bf16 v[24:27], v[124:127], a[180:183], v[24:27]\n\t" \
  "ds_read_b64_tr_b16 v[140:141], v162 offset:8192\n\t" \
  "v_mfma_f32_16x16x32_bf16 v[24:27], v[128:131], a[184:187], v[24:27]\n\t" \
  "v_exp_f32 v13, v13\n\t" \
  "v_mfma_f32_16x16x32_bf16 v[24:27], v[132:135], a[188:191], v[24:27]\n\t" \
  "ds_read_b64_tr_b16 v[142:143], v162 offset:12288\n\t" \
  "v_cvt_pk_bf16_f32 v112, v12, v13\n\t" \
  "v_mfma_f32_16x16x32_bf16 v[28:31], v[120:123], a[192:195], 0\n\t" \
  "v_exp_f32 v14, v14\n\t" \
  "v_mfma_f32_16x16x32_bf16 v[28:31], v[124:127], a[196:199], v[28:31]\n\t" \
  "ds_read_b64_tr_b16 v[144:145], v163 offset:0\n\t" \
  "v_mfma_f32_16x16x32_bf16 v[28:31], v[128:131], a[200:203], v[28:31]\n\t" \
  "v_exp_f32 v15, v15\n\t" \
  "v_mfma_f32_16x16x32_bf16 v[28:31], v[132:135], a[204:207], v[28:31]\n\t" \
  "ds_read_b64_tr_b16 v[146:147], v163 offset:4096\n\t" \
  "v_cvt_pk_bf16_f32 v113, v14, v15\n\t" \
  "v_mfma_f32_16x16x32_bf16 v[32:35], v[120:123], a[208:211], 0\n\t" \
  "v_exp_f32 v16, v16\n\t" \
  "v_mfma_f32_16x16x32_bf16 v[32:35], v[124:127], a[212:215], v[32:35]\n\t" \
  "ds_read_b64_tr_b16 v[148:149], v163 offset:8192\n\t" \
  "v_mfma_f32_16x16x32_bf16 v[32:35], v[128:131], a[216:219], v[32:35]\n\t" \
  "v_exp_f32 v17, v17\n\t" \
  "v_mfma_f32_16x16x32_bf16 v[32:35], v[132:135], a[220:223], v[32:35]\n\t" \
  "ds_read_b64_tr_b16 v[150:151], v163 offset:12288\n\t" \
  "v_cvt_pk_bf16_f32 v116, v16, v17\n\t" \
  "v_mfma_f32_16x16x32_bf16 v[36:39], v[120:123], a[224:227], 0\n\t" \
  "v_exp_f32 v18, v18\n\t" \
  "v_mfma_f32_16x16x32_bf16 v[36:39], v[124:127], a[228:231], v[36:39]\n\t" \
  "v_mfma_f32_16x16x32_bf16 v[36:39], v[128:131], a[232:235], v[36:39]\n\t" \
  "s_waitcnt lgkmcnt(4)\n\t" \
  "v_exp_f32 v19, v19\n\t" \
  "v_mfma_f32_16x16x32_bf16 v[36:39], v[132:135], a[236:239], v[36:39]\n\t" \
  "v_cvt_pk_bf16_f32 v117, v18, v19\n\t" \
  "v_mfma_f32_16x16x32_bf16 a[120:123], v[136:139], v[40:43], 0\n\t" \
  "v_exp_f32 v20, v20\n\t" \
  "v_mfma_f32_16x16x32_bf16 a[120:123], v[140:143], v[60:63], a[120:123]\n\t" \
  "v_mfma_f32_16x16x32_bf16 a[124:127], v[136:139], v[44:47], 0\n\t" \
  "v_exp_f32 v21, v21\n\t" \
  "v_mfma_f32_16x16x32_bf16 a[124:127], v[140:143], v[64:67], a[124:127]\n\t" \
  "v_mfma_f32_16x16x32_bf16 a[128:131], v[136:139], v[48:51], 0\n\t" \
  "s_waitcnt lgkmcnt(0)\n\t" \
  "v_exp_f32 v22, v22\n\t" \
  "v_mfma_f32_16x16x32_bf16 a[128:131], v[140:143], v[68:71], a[128:131]\n\t" \
  "v_mfma_f32_16x16x32_bf16 a[132:135], v[136:139], v[52:55], 0\n\t" \
  "s_waitcnt vmcnt(0)\n\t" \
  "s_barrier\n\t" \
  "v_exp_f32 v23, v23\n\t" \
  "v_mfma_f32_16x16x32_bf16 a[132:135], v[140:143], v[72:75], a[132:135]\n\t" \
  "ds_read_b128 v[120:123], v152 offset:32768\n\t" \
  "v_cvt_pk_bf16_f32 v102, v20, v21\n\t" \
  "v_mfma_f32_16x16x32_bf16 a[136:139], v[136:139], v[56:59], 0\n\t" \
  "v_exp_f32 v24, v24\n\t" \
  "v_mfma_f32_16x16x32_bf16 a[136:139], v[140:143], v[76:79], a[136:139]\n\t" \
  "ds_read_b128 v[124:127], v153 offset:32768\n\t" \
  "v_cvt_pk_bf16_f32 v103, v22, v23\n\t" \
  "v_mfma_f32_16x16x32_bf16 a[140:143], v[144:147], v[40:43], 0\n\t" \
  "v_exp_f32 v25, v25\n\t" \
  "v_mfma_f32_16x16x32_bf16 a[140:143], v[148:151], v[60:63], a[140:143]\n\t" \
  "ds_read_b128 v[128:131], v154 offset:32768\n\t" \
  "v_cvt_pk_bf16_f32 v106, v24, v25\n\t" \
  "v_mfma_f32_16x16x32_bf16 a[144:147], v[144:147], v[44:47], 0\n\t" \
  "v_exp_f32 v26, v26\n\t" \
  "v_mfma_f32_16x16x32_bf16 a[144:147], v[148:151], v[64:67], a[144:147]\n\t" \
  "ds_read_b128 v[132:135], v155 offset:32768\n\t" \
  "v_mfma_f32_16x16x32_bf16 a[148:151], v[144:147], v[48:51], 0\n\t" \
  "v_exp_f32 v27, v27\n\t" \
  "v_mfma_f32_16x16x32_bf16 a[148:151], v[148:151], v[68:71], a[148:151]\n\t" \
  "v_cvt_pk_bf16_f32 v107, v26, v27\n\t" \
  "v_mfma_f32_16x16x32_bf16 a[152:155], v[144:147], v[52:55], 0\n\t" \
  "v_exp_f32 v28, v28\n\t" \
  "v_mfma_f32_16x16x32_bf16 a[152:155], v[148:151], v[72:75], a[152:155]\n\t" \
  "v_mfma_f32_16x16x32_bf16 a[156:159], v[144:147], v[56:59], 0\n\t" \
  "s_waitcnt lgkmcnt(0)\n\t" \
  "v_exp_f32 v29, v29\n\t" \
  "v_mfma_f32_16x16x32_bf16 a[156:159], v[148:151], v[76:79], a[156:159]\n\t" \
  "v_cvt_pk_bf16_f32 v110, v28, v29\n\t" \
  "v_add_u32 v236, s74, v172\n\t" \
  "ds_read_b128 v[220:223], v236 offset:0\n\t" \
  "ds_read_b128 v[224:227], v236 offset:1024\n\t" \
  "ds_read_b128 v[228:231], v236 offset:2048\n\t" \
  "ds_read_b128 v[232:235], v236 offset:3072\n\t" \
  "v_mov_b32 v240, 16\n\t" \
  "v_mov_b32 v241, 16\n\t" \
  "v_mov_b32 v242, 16\n\t" \
  "v_mov_b32 v243, 16\n\t" \
  "v_lshlrev_b32 v237, 1, v172\n\t" \
  "v_add_u32 v237, s74, v237\n\t" \
  "ds_write_b128 v237, v[240:243] offset:4096\n\t" \
  "ds_write_b128 v237, v[240:243] offset:4112\n\t" \
  "v_lshrrev_b32 v238, 4, v172\n\t" \
  "v_and_b32 v239, 15, v238\n\t" \
  "v_lshrrev_b32 v238, 4, v238\n\t" \
  "v_lshlrev_b32 v239, 7, v239\n\t" \
  "v_lshl_add_u32 v239, v238, 5, v239\n\t" \
  "v_add_u32 v239, s74, v239\n\t" \
  "v_and_b32 v238, 1, v238\n\t" \
  "v_lshlrev_b32 v238, 7, v238\n\t" \
  "s_brev_b32 s89, 1\n\t" \
  "v_subrev_u32 v240, 1, v250\n\t" \
  "v_subrev_u32 v241, 2, v250\n\t" \
  "v_and_or_b32 v240, v240, s89, v251\n\t" \
  "v_and_or_b32 v241, v241, s89, v251\n\t" \
  "s_waitcnt lgkmcnt(0)\n\t" \
  "ds_write_b32 v240, v220 offset:0\n\t" \
  "ds_write_b32 v241, v221 offset:4\n\t" \
  "v_subrev_u32 v240, 3, v250\n\t" \
  "v_subrev_u32 v241, 4, v250\n\t" \
  "v_and_or_b32 v240, v240, s89, v251\n\t" \
  "v_and_or_b32 v241, v241, s89, v251\n\t" \
  "ds_write_b32 v240, v222 offset:8\n\t" \
  "ds_write_b32 v241, v223 offset:12\n\t" \
  "v_subrev_u32 v240, 5, v250\n\t" \
  "v_subrev_u32 v241, 6, v250\n\t" \
  "v_and_or_b32 v240, v240, s89, v251\n\t" \
  "v_and_or_b32 v241, v241, s89, v251\n\t" \
  "ds_write_b32 v240, v224 offset:16\n\t" \
  "ds_write_b32 v241, v225 offset:20\n\t" \
  "v_subrev_u32 v240, 7, v250\n\t" \
  "v_subrev_u32 v241, 8, v250\n\t" \
  "v_and_or_b32 v240, v240, s89, v251\n\t" \
  "v_and_or_b32 v241, v241, s89, v251\n\t" \
  "ds_write_b32 v240, v226 offset:24\n\t" \
  "ds_write_b32 v241, v227 offset:28\n\t" \
  "v_subrev_u32 v240, 9, v250\n\t" \
  "v_subrev_u32 v241, 10, v250\n\t" \
  "v_and_or_b32 v240, v240, s89, v251\n\t" \
  "v_and_or_b32 v241, v241, s89, v251\n\t" \
  "ds_write_b32 v240, v228 offset:32\n\t" \
  "ds_write_b32 v241, v229 offset:36\n\t" \
  "v_subrev_u32 v240, 11, v250\n\t" \
  "v_subrev_u32 v241, 12, v250\n\t" \
  "v_and_or_b32 v240, v240, s89, v251\n\t" \
  "v_and_or_b32 v241, v241, s89, v251\n\t" \
  "ds_write_b32 v240, v230 offset:40\n\t" \
  "ds_write_b32 v241, v231 offset:44\n\t" \
  "v_subrev_u32 v240, 13, v250\n\t" \
  "v_subrev_u32 v241, 14, v250\n\t" \
  "v_and_or_b32 v240, v240, s89, v251\n\t" \
  "v_and_or_b32 v241, v241, s89, v251\n\t" \
  "ds_write_b32 v240, v232 offset:48\n\t" \
  "ds_write_b32 v241, v233 offset:52\n\t" \
  "v_subrev_u32 v240, 15, v250\n\t" \
  "v_subrev_u32 v241, 16, v250\n\t" \
  "v_and_or_b32 v240, v240, s89, v251\n\t" \
  "v_and_or_b32 v241, v241, s89, v251\n\t" \
  "ds_write_b32 v240, v234 offset:56\n\t" \
  "ds_write_b32 v241, v235 offset:60\n\t" \
  "v_mov_b32 v240, 0\n\t" \
  "v_mov_b32 v241, 0\n\t" \
  "v_mov_b32 v242, 0\n\t" \
  "v_mov_b32 v243, 0\n\t" \
  "v_mov_b32 v244, 0xffffffe0\n\t" \
  "v_mov_b32 v245, 1\n\t" \
  "v_mov_b32 v250, 0xffff\n\t" \
  "s_waitcnt lgkmcnt(0)\n\t" \
  "ds_read_b128 v[220:223], v239 offset:4096\n\t" \
  "ds_read_b128 v[224:227], v239 offset:4112\n\t" \
  "s_waitcnt lgkmcnt(0)\n\t" \
  "ds_write_b128 v236, v[240:243] offset:0\n\t" \
  "ds_write_b128 v236, v[240:243] offset:1024\n\t" \
  "ds_write_b128 v236, v[240:243] offset:2048\n\t" \
  "ds_write_b128 v236, v[240:243] offset:3072\n\t" \
  "ds_write_b128 v236, v[240:243] offset:4096\n\t" \
  "ds_write_b128 v236, v[240:243] offset:5120\n\t" \
  "v_xor_b32 v237, 0, v238\n\t" \
  "v_xor_b32 v237, v237, v220\n\t" \
  "v_and_b32 v208, v237, v244\n\t" \
  "v_xor_b32 v237, 32, v238\n\t" \
  "v_xor_b32 v237, v237, v221\n\t" \
  "v_and_b32 v209, v237, v244\n\t" \
  "v_xor_b32 v237, 64, v238\n\t" \
  "v_xor_b32 v237, v237, v222\n\t" \
  "v_and_b32 v210, v237, v244\n\t" \
  "v_xor_b32 v237, 96, v238\n\t" \
  "v_xor_b32 v237, v237, v223\n\t" \
  "v_and_b32 v211, v237, v244\n\t" \
  "v_xor_b32 v237, 0, v238\n\t" \
  "v_xor_b32 v237, v237, v224\n\t" \
  "v_and_b32 v212, v237, v244\n\t" \
  "v_xor_b32 v237, 32, v238\n\t" \
  "v_xor_b32 v237, v237, v225\n\t" \
  "v_and_b32 v213, v237, v244\n\t" \
  "v_xor_b32 v237, 64, v238\n\t" \
  "v_xor_b32 v237, v237, v226\n\t" \
  "v_and_b32 v214, v237, v244\n\t" \
  "v_xor_b32 v237, 96, v238\n\t" \
  "v_xor_b32 v237, v237, v227\n\t" \
  "v_and_b32 v215, v237, v244\n\t" \
  "v_lshlrev_b32 v237, v220, v245\n\t" \
  "v_lshlrev_b32 v239, v221, v245\n\t" \
  "v_and_b32 v237, v237, v250\n\t" \
  "v_lshl_or_b32 v216, v239, 16, v237\n\t" \
  "v_lshlrev_b32 v237, v222, v245\n\t" \
  "v_lshlrev_b32 v239, v223, v245\n\t" \
  "v_and_b32 v237, v237, v250\n\t" \
  "v_lshl_or_b32 v217, v239, 16, v237\n\t" \
  "v_lshlrev_b32 v237, v224, v245\n\t" \
  "v_lshlrev_b32 v239, v225, v245\n\t" \
  "v_and_b32 v237, v237, v250\n\t" \
  "v_lshl_or_b32 v218, v239, 16, v237\n\t" \
  "v_lshlrev_b32 v237, v226, v245\n\t" \
  "v_lshlrev_b32 v239, v227, v245\n\t" \
  "v_and_b32 v237, v237, v250\n\t" \
  "v_lshl_or_b32 v219, v239, 16, v237\n\t" \
  "v_mov_b32 v220, 0\n\t" \
  "v_mov_b32 v221, 0\n\t" \
  "v_mov_b32 v222, 0\n\t" \
  "v_mov_b32 v223, 0\n\t" \
  "v_mov_b32 v224, 0\n\t" \
  "v_mov_b32 v225, 0\n\t" \
  "v_mov_b32 v226, 0\n\t" \
  "v_mov_b32 v227, 0\n\t" \
  "v_mov_b32 v228, 0\n\t" \
  "v_mov_b32 v229, 0\n\t" \
  "v_mov_b32 v230, 0\n\t" \
  "v_mov_b32 v231, 0\n\t" \
  "v_mov_b32 v232, 0\n\t" \
  "v_mov_b32 v233, 0\n\t" \
  "v_mov_b32 v234, 0\n\t" \
  "v_mov_b32 v235, 0\n\t" \
  "v_mov_b32 v236, 0\n\t" \
  "v_mov_b32 v237, 0\n\t" \
  "v_mov_b32 v238, 0\n\t" \
  "v_mov_b32 v239, 0\n\t" \
  "s_waitcnt vmcnt(0) lgkmcnt(0)\n\t" \
  "s_mov_b32 s88, 3\n\t" \
  "1:\n\t" \
  "v_mfma_f32_16x16x32_bf16 v[0:3], v[120:123], a[160:163], 0\n\t" \
  "v_exp_f32 v30, v30\n\t" \
  "v_mfma_f32_16x16x32_bf16 v[0:3], v[124:127], a[164:167], v[0:3]\n\t" \
  "ds_read_b64_tr_b16 v[136:137], v156 offset:16384\n\t" \
  "v_mov_b32_dpp v244, v216 row_newbcast:0 row_mask:0xf bank_mask:0xf\n\t" \
  "v_mfma_f32_16x16x32_bf16 v[0:3], v[128:131], a[168:171], v[0:3]\n\t" \
  "v_exp_f32 v31, v31\n\t" \
  "v_mfma_f32_16x16x32_bf16 v[0:3], v[132:135], a[172:175], v[0:3]\n\t" \
  "ds_read_b64_tr_b16 v[138:139], v156 offset:20480\n\t" \
  "v_cvt_pk_bf16_f32 v111, v30, v31\n\t" \
  "v_mfma_f32_16x16x32_bf16 v[4:7], v[120:123], a[176:179], 0\n\t" \
  "v_exp_f32 v32, v32\n\t" \
  "v_mfma_f32_16x16x32_bf16 v[4:7], v[124:127], a[180:183], v[4:7]\n\t" \
  "ds_read_b64_tr_b16 v[140:141], v156 offset:24576\n\t" \
  "v_alignbit_b32 v244, v244, v244, v248\n\t" \
  "v_mfma_f32_16x16x32_bf16 v[4:7], v[128:131], a[184:187], v[4:7]\n\t" \
  "v_exp_f32 v33, v33\n\t" \
  "v_mfma_f32_16x16x32_bf16 v[4:7], v[132:135], a[188:191], v[4:7]\n\t" \
  "ds_read_b64_tr_b16 v[142:143], v156 offset:28672\n\t" \
  "v_cvt_pk_bf16_f32 v114, v32, v33\n\t" \
  "v_mfma_f32_16x16x32_bf16 v[8:11], v[120:123], a[192:195], 0\n\t" \
  "v_exp_f32 v34, v34\n\t" \
  "v_mfma_f32_16x16x32_bf16 v[8:11], v[124:127], a[196:199], v[8:11]\n\t" \
  "ds_read_b64_tr_b16 v[144:145], v157 offset:16384\n\t" \
  "v_and_b32 v220, v244, v249\n\t" \
  "v_mfma_f32_16x16x32_bf16 v[8:11], v[128:131], a[200:203], v[8:11]\n\t" \
  "v_exp_f32 v35, v35\n\t" \
  "v_mfma_f32_16x16x32_bf16 v[8:11], v[132:135], a[204:207], v[8:11]\n\t" \
  "ds_read_b64_tr_b16 v[146:147], v157 offset:20480\n\t" \
  "v_cvt_pk_bf16_f32 v115, v34, v35\n\t" \
  "v_mfma_f32_16x16x32_bf16 v[12:15], v[120:123], a[208:211], 0\n\t" \
  "v_exp_f32 v36, v36\n\t" \
  "v_mfma_f32_16x16x32_bf16 v[12:15], v[124:127], a[212:215], v[12:15]\n\t" \
  "ds_read_b64_tr_b16 v[148:149], v157 offset:24576\n\t" \
  "v_mov_b32_dpp v245, v217 row_newbcast:0 row_mask:0xf bank_mask:0xf\n\t" \
  "v_mfma_f32_16x16x32_bf16 v[12:15], v[128:131], a[216:219], v[12:15]\n\t" \
  "v_exp_f32 v37, v37\n\t" \
  "v_mfma_f32_16x16x32_bf16 v[12:15], v[132:135], a[220:223], v[12:15]\n\t" \
  "ds_read_b64_tr_b16 v[150:151], v157 offset:28672\n\t" \
  "v_cvt_pk_bf16_f32 v118, v36, v37\n\t" \
  "v_mfma_f32_16x16x32_bf16 v[16:19], v[120:123], a[224:227], 0\n\t" \
  "s_waitcnt lgkmcnt(8)\n\t" \
  "v_exp_f32 v38, v38\n\t" \
  "v_mfma_f32_16x16x32_bf16 v[16:19], v[124:127], a[228:231], v[16:19]\n\t" \
  "ds_read_b64_tr_b16 v[236:237], v169 offset:0\n\t" \
  "v_alignbit_b32 v245, v245, v245, v248\n\t" \
  "v_mfma_f32_16x16x32_bf16 v[16:19], v[128:131], a[232:235], v[16:19]\n\t" \
  "s_waitcnt lgkmcnt(5)\n\t" \
  "v_exp_f32 v39, v39\n\t" \
  "v_mfma_f32_16x16x32_bf16 v[16:19], v[132:135], a[236:239], v[16:19]\n\t" \
  "v_cvt_pk_bf16_f32 v119, v38, v39\n\t" \
  "v_and_b32 v221, v245, v249\n\t" \
  "v_mfma_f32_16x16x32_bf16 v[192:195], v[232:235], v[224:227], v[192:195]\n\t" \
  "ds_read_b64_tr_b16 v[238:239], v169 offset:4096\n\t" \
  "v_mfma_f32_16x16x32_bf16 a[0:3], v[136:139], v[80:83], a[0:3]\n\t" \
  "v_exp_f32 v0, v0\n\t" \
  "v_mfma_f32_16x16x32_bf16 a[0:3], v[140:143], v[100:103], a[0:3]\n\t" \
  "ds_read_b128 v[120:123], v152 offset:36864\n\t" \
  "v_mfma_f32_16x16x32_bf16 a[4:7], v[136:139], v[84:87], a[4:7]\n\t" \
  "v_exp_f32 v1, v1\n\t" \
  "v_mfma_f32_16x16x32_bf16 a[4:7], v[140:143], v[104:107], a[4:7]\n\t" \
  "ds_read_b128 v[124:127], v153 offset:36864\n\t" \
  "v_cvt_pk_bf16_f32 v40, v0, v1\n\t" \
  "v_mfma_f32_16x16x32_bf16 a[8:11], v[136:139], v[88:91], a[8:11]\n\t" \
  "s_waitcnt lgkmcnt(4)\n\t" \
  "v_exp_f32 v2, v2\n\t" \
  "v_mfma_f32_16x16x32_bf16 a[8:11], v[140:143], v[108:111], a[8:11]\n\t" \
  "ds_read_b128 v[128:131], v154 offset:36864\n\t" \
  "v_mfma_f32_16x16x32_bf16 a[12:15], v[136:139], v[92:95], a[12:15]\n\t" \
  "s_add_u32 %[soff], %[soff], 12288\n\t" \
  "v_exp_f32 v3, v3\n\t" \
  "v_mfma_f32_16x16x32_bf16 a[12:15], v[140:143], v[112:115], a[12:15]\n\t" \
  "ds_read_b128 v[132:135], v155 offset:36864\n\t" \
  "v_cvt_pk_bf16_f32 v41, v2, v3\n\t" \
  "v_mfma_f32_16x16x32_bf16 a[16:19], v[136:139], v[96:99], a[16:19]\n\t" \
  "s_add_u32 m0, s72, 0\n\t" \
  "v_exp_f32 v4, v4\n\t" \
  "v_mfma_f32_16x16x32_bf16 a[16:19], v[140:143], v[116:119], a[16:19]\n\t" \
  "buffer_load_dwordx4 v172, %[srd], %[soff] offen offset:0 lds\n\t" \
  "v_mfma_f32_16x16x32_bf16 a[20:23], v[144:147], v[80:83], a[20:23]\n\t" \
  "v_exp_f32 v5, v5\n\t" \
  "v_mfma_f32_16x16x32_bf16 a[20:23], v[148:151], v[100:103], a[20:23]\n\t" \
  "buffer_load_dwordx4 v172, %[srd], %[soff] offen offset:1024 lds\n\t" \
  "v_mfma_f32_16x16x32_bf16 a[24:27], v[144:147], v[84:87], a[24:27]\n\t" \
  "v_exp_f32 v6, v6\n\t" \
  "v_mfma_f32_16x16x32_bf16 a[24:27], v[148:151], v[104:107], a[24:27]\n\t" \
  "buffer_load_dwordx4 v172, %[srd], %[soff] offen offset:2048 lds\n\t" \
  "v_mfma_f32_16x16x32_bf16 a[28:31], v[144:147], v[88:91], a[28:31]\n\t" \
  "v_exp_f32 v7, v7\n\t" \
  "v_mfma_f32_16x16x32_bf16 a[28:31], v[148:151], v[108:111], a[28:31]\n\t" \
  "v_cvt_pk_bf16_f32 v44, v4, v5\n\t" \
  "v_cvt_pk_bf16_f32 v45, v6, v7\n\t" \
  "v_mfma_f32_16x16x32_bf16 a[32:35], v[144:147], v[92:95], a[32:35]\n\t" \
  "s_waitcnt lgkmcnt(4)\n\t" \
  "v_exp_f32 v8, v8\n\t" \
  "v_mfma_f32_16x16x32_bf16 a[32:35], v[148:151], v[112:115], a[32:35]\n\t" \
  "ds_read_b64_tr_b16 v[232:233], v170 offset:0\n\t" \
  "v_mfma_f32_16x16x32_bf16 a[36:39], v[144:147], v[96:99], a[36:39]\n\t" \
  "s_waitcnt lgkmcnt(1)\n\t" \
  "v_exp_f32 v9, v9\n\t" \
  "v_mfma_f32_16x16x32_bf16 a[36:39], v[148:151], v[116:119], a[36:39]\n\t" \
  "buffer_load_dwordx4 v172, %[srd], %[soff] offen offset:3072 lds\n\t" \
  "v_mfma_f32_16x16x32_bf16 v[196:199], v[236:239], v[224:227], v[196:199]\n\t" \
  "ds_read_b64_tr_b16 v[234:235], v170 offset:4096\n\t" \
  "v_cvt_pk_bf16_f32 v48, v8, v9\n\t" \
  "v_mfma_f32_16x16x32_bf16 v[20:23], v[120:123], a[160:163], 0\n\t" \
  "v_exp_f32 v10, v10\n\t" \
  "v_mfma_f32_16x16x32_bf16 v[20:23], v[124:127], a[164:167], v[20:23]\n\t" \
  "ds_read_b64_tr_b16 v[136:137], v158 offset:16384\n\t" \
  "v_mov_b32_dpp v244, v218 row_newbcast:0 row_mask:0xf bank_mask:0xf\n\t" \
  "v_mfma_f32_16x16x32_bf16 v[20:23], v[128:131], a[168:171], v[20:23]\n\t" \
  "v_exp_f32 v11, v11\n\t" \
  "v_mfma_f32_16x16x32_bf16 v[20:23], v[132:135], a[172:175], v[20:23]\n\t" \
  "ds_read_b64_tr_b16 v[138:139], v158 offset:20480\n\t" \
  "v_cvt_pk_bf16_f32 v49, v10, v11\n\t" \
  "v_mfma_f32_16x16x32_bf16 v[24:27], v[120:123], a[176:179], 0\n\t" \
  "v_exp_f32 v12, v12\n\t" \
  "v_mfma_f32_16x16x32_bf16 v[24:27], v[124:127], a[180:183], v[24:27]\n\t" \
  "ds_read_b64_tr_b16 v[140:141], v158 offset:24576\n\t" \
  "v_alignbit_b32 v244, v244, v244, v248\n\t" \
  "v_mfma_f32_16x16x32_bf16 v[24:27], v[128:131], a[184:187], v[24:27]\n\t" \
  "v_exp_f32 v13, v13\n\t" \
  "v_mfma_f32_16x16x32_bf16 v[24:27], v[132:135], a[188:191], v[24:27]\n\t" \
  "ds_read_b64_tr_b16 v[142:143], v158 offset:28672\n\t" \
  "v_cvt_pk_bf16_f32 v52, v12, v13\n\t" \
  "v_mfma_f32_16x16x32_bf16 v[28:31], v[120:123], a[192:195], 0\n\t" \
  "v_exp_f32 v14, v14\n\t" \
  "v_mfma_f32_16x16x32_bf16 v[28:31], v[124:127], a[196:199], v[28:31]\n\t" \
  "ds_read_b64_tr_b16 v[144:145], v159 offset:16384\n\t" \
  "v_and_b32 v222, v244, v249\n\t" \
  "v_mfma_f32_16x16x32_bf16 v[28:31], v[128:131], a[200:203], v[28:31]\n\t" \
  "v_exp_f32 v15, v15\n\t" \
  "v_mfma_f32_16x16x32_bf16 v[28:31], v[132:135], a[204:207], v[28:31]\n\t" \
  "ds_read_b64_tr_b16 v[146:147], v159 offset:20480\n\t" \
  "v_cvt_pk_bf16_f32 v53, v14, v15\n\t" \
  "v_mfma_f32_16x16x32_bf16 v[32:35], v[120:123], a[208:211], 0\n\t" \
  "v_exp_f32 v16, v16\n\t" \
  "v_mfma_f32_16x16x32_bf16 v[32:35], v[124:127], a[212:215], v[32:35]\n\t" \
  "ds_read_b64_tr_b16 v[148:149], v159 offset:24576\n\t" \
  "v_mov_b32_dpp v245, v219 row_newbcast:0 row_mask:0xf bank_mask:0xf\n\t" \
  "v_mfma_f32_16x16x32_bf16 v[32:35], v[128:131], a[216:219], v[32:35]\n\t" \
  "v_exp_f32 v17, v17\n\t" \
  "v_mfma_f32_16x16x32_bf16 v[32:35], v[132:135], a[220:223], v[32:35]\n\t" \
  "ds_read_b64_tr_b16 v[150:151], v159 offset:28672\n\t" \
  "v_cvt_pk_bf16_f32 v56, v16, v17\n\t" \
  "v_mfma_f32_16x16x32_bf16 v[36:39], v[120:123], a[224:227], 0\n\t" \
  "s_waitcnt lgkmcnt(8)\n\t" \
  "v_exp_f32 v18, v18\n\t" \
  "v_mfma_f32_16x16x32_bf16 v[36:39], v[124:127], a[228:231], v[36:39]\n\t" \
  "ds_read_b64_tr_b16 v[236:237], v171 offset:0\n\t" \
  "v_alignbit_b32 v245, v245, v245, v248\n\t" \
  "v_mfma_f32_16x16x32_bf16 v[36:39], v[128:131], a[232:235], v[36:39]\n\t" \
  "s_waitcnt lgkmcnt(5)\n\t" \
  "v_exp_f32 v19, v19\n\t" \
  "v_mfma_f32_16x16x32_bf16 v[36:39], v[132:135], a[236:239], v[36:39]\n\t" \
  "v_cvt_pk_bf16_f32 v57, v18, v19\n\t" \
  "v_and_b32 v223, v245, v249\n\t" \
  "v_mfma_f32_16x16x32_bf16 v[200:203], v[232:235], v[224:227], v[200:203]\n\t" \
  "ds_read_b64_tr_b16 v[238:239], v171 offset:4096\n\t" \
  "v_mfma_f32_16x16x32_bf16 a[40:43], v[136:139], v[80:83], a[40:43]\n\t" \
  "v_exp_f32 v20, v20\n\t" \
  "v_mfma_f32_16x16x32_bf16 a[40:43], v[140:143], v[100:103], a[40:43]\n\t" \
  "ds_read_b128 v[120:123], v152 offset:40960\n\t" \
  "v_xor_b32_dpp v240, v208, v247 row_newbcast:0 row_mask:0xf bank_mask:0xf\n\t" \
  "v_mfma_f32_16x16x32_bf16 a[44:47], v[136:139], v[84:87], a[44:47]\n\t" \
  "v_exp_f32 v21, v21\n\t" \
  "v_mfma_f32_16x16x32_bf16 a[44:47], v[140:143], v[104:107], a[44:47]\n\t" \
  "ds_read_b128 v[124:127], v153 offset:40960\n\t" \
  "v_cvt_pk_bf16_f32 v42, v20, v21\n\t" \
  "v_mfma_f32_16x16x32_bf16 a[48:51], v[136:139], v[88:91], a[48:51]\n\t" \
  "s_waitcnt lgkmcnt(4)\n\t" \
  "v_exp_f32 v22, v22\n\t" \
  "v_mfma_f32_16x16x32_bf16 a[48:51], v[140:143], v[108:111], a[48:51]\n\t" \
  "ds_read_b128 v[128:131], v154 offset:40960\n\t" \
  "v_xor_b32_dpp v241, v209, v247 row_newbcast:0 row_mask:0xf bank_mask:0xf\n\t" \
  "v_mfma_f32_16x16x32_bf16 a[52:55], v[136:139], v[92:95], a[52:55]\n\t" \
  "s_add_u32 %[soff], %[soff], 4096\n\t" \
  "v_exp_f32 v23, v23\n\t" \
  "v_mfma_f32_16x16x32_bf16 a[52:55], v[140:143], v[112:115], a[52:55]\n\t" \
  "ds_read_b128 v[132:135], v155 offset:40960\n\t" \
  "v_cvt_pk_bf16_f32 v43, v22, v23\n\t" \
  "v_mfma_f32_16x16x32_bf16 a[56:59], v[136:139], v[96:99], a[56:59]\n\t" \
  "s_add_u32 m0, s72, 4096\n\t" \
  "v_exp_f32 v24, v24\n\t" \
  "v_mfma_f32_16x16x32_bf16 a[56:59], v[140:143], v[116:119], a[56:59]\n\t" \
  "buffer_load_dwordx4 v172, %[srd], %[soff] offen offset:0 lds\n\t" \
  "v_mfma_f32_16x16x32_bf16 a[60:63], v[144:147], v[80:83], a[60:63]\n\t" \
  "v_exp_f32 v25, v25\n\t" \
  "v_mfma_f32_16x16x32_bf16 a[60:63], v[148:151], v[100:103], a[60:63]\n\t" \
  "buffer_load_dwordx4 v172, %[srd], %[soff] offen offset:1024 lds\n\t" \
  "v_mfma_f32_16x16x32_bf16 a[64:67], v[144:147], v[84:87], a[64:67]\n\t" \
  "v_exp_f32 v26, v26\n\t" \
  "v_mfma_f32_16x16x32_bf16 a[64:67], v[148:151], v[104:107], a[64:67]\n\t" \
  "buffer_load_dwordx4 v172, %[srd], %[soff] offen offset:2048 lds\n\t" \
  "v_mfma_f32_16x16x32_bf16 a[68:71], v[144:147], v[88:91], a[68:71]\n\t" \
  "s_waitcnt vmcnt(7)\n\t" \
  "v_exp_f32 v27, v27\n\t" \
  "v_mfma_f32_16x16x32_bf16 a[68:71], v[148:151], v[108:111], a[68:71]\n\t" \
  "v_cvt_pk_bf16_f32 v46, v24, v25\n\t" \
  "v_cvt_pk_bf16_f32 v47, v26, v27\n\t" \
  "v_mfma_f32_16x16x32_bf16 a[72:75], v[144:147], v[92:95], a[72:75]\n\t" \
  "s_waitcnt lgkmcnt(4)\n\t" \
  "v_exp_f32 v28, v28\n\t" \
  "v_mfma_f32_16x16x32_bf16 a[72:75], v[148:151], v[112:115], a[72:75]\n\t" \
  "ds_read_b64_tr_b16 v[232:233], v164 offset:8192\n\t" \
  "v_xor_b32_dpp v242, v210, v247 row_newbcast:0 row_mask:0xf bank_mask:0xf\n\t" \
  "v_mfma_f32_16x16x32_bf16 a[76:79], v[144:147], v[96:99], a[76:79]\n\t" \
  "s_waitcnt lgkmcnt(1)\n\t" \
  "v_exp_f32 v29, v29\n\t" \
  "v_mfma_f32_16x16x32_bf16 a[76:79], v[148:151], v[116:119], a[76:79]\n\t" \
  "buffer_load_dwordx4 v172, %[srd], %[soff] offen offset:3072 lds\n\t" \
  "v_mfma_f32_16x16x32_bf16 v[204:207], v[236:239], v[224:227], v[204:207]\n\t" \
  "ds_read_b64_tr_b16 v[234:235], v164 offset:12288\n\t" \
  "v_cvt_pk_bf16_f32 v50, v28, v29\n\t" \
  "v_mfma_f32_16x16x32_bf16 v[0:3], v[120:123], a[160:163], 0\n\t" \
  "v_exp_f32 v30, v30\n\t" \
  "v_mfma_f32_16x16x32_bf16 v[0:3], v[124:127], a[164:167], v[0:3]\n\t" \
  "ds_read_b64_tr_b16 v[136:137], v160 offset:16384\n\t" \
  "v_xor_b32_dpp v243, v211, v247 row_newbcast:0 row_mask:0xf bank_mask:0xf\n\t" \
  "v_mfma_f32_16x16x32_bf16 v[0:3], v[128:131], a[168:171], v[0:3]\n\t" \
  "v_exp_f32 v31, v31\n\t" \
  "v_mfma_f32_16x16x32_bf16 v[0:3], v[132:135], a[172:175], v[0:3]\n\t" \
  "ds_read_b64_tr_b16 v[138:139], v160 offset:20480\n\t" \
  "v_cvt_pk_bf16_f32 v51, v30, v31\n\t" \
  "v_mfma_f32_16x16x32_bf16 v[4:7], v[120:123], a[176:179], 0\n\t" \
  "v_exp_f32 v32, v32\n\t" \
  "v_mfma_f32_16x16x32_bf16 v[4:7], v[124:127], a[180:183], v[4:7]\n\t" \
  "ds_read_b64_tr_b16 v[140:141], v160 offset:24576\n\t" \
  "v_mfma_f32_16x16x32_bf16 v[4:7], v[128:131], a[184:187], v[4:7]\n\t" \
  "v_exp_f32 v33, v33\n\t" \
  "v_mfma_f32_16x16x32_bf16 v[4:7], v[132:135], a[188:191], v[4:7]\n\t" \
  "ds_read_b64_tr_b16 v[142:143], v160 offset:28672\n\t" \
  "v_cvt_pk_bf16_f32 v54, v32, v33\n\t" \
  "v_mfma_f32_16x16x32_bf16 v[8:11], v[120:123], a[192:195], 0\n\t" \
  "v_exp_f32 v34, v34\n\t" \
  "v_mfma_f32_16x16x32_bf16 v[8:11], v[124:127], a[196:199], v[8:11]\n\t" \
  "ds_read_b64_tr_b16 v[144:145], v161 offset:16384\n\t" \
  "v_mfma_f32_16x16x32_bf16 v[8:11], v[128:131], a[200:203], v[8:11]\n\t" \
  "v_exp_f32 v35, v35\n\t" \
  "v_mfma_f32_16x16x32_bf16 v[8:11], v[132:135], a[204:207], v[8:11]\n\t" \
  "ds_read_b64_tr_b16 v[146:147], v161 offset:20480\n\t" \
  "v_cvt_pk_bf16_f32 v55, v34, v35\n\t" \
  "v_mfma_f32_16x16x32_bf16 v[12:15], v[120:123], a[208:211], 0\n\t" \
  "v_exp_f32 v36, v36\n\t" \
  "v_mfma_f32_16x16x32_bf16 v[12:15], v[124:127], a[212:215], v[12:15]\n\t" \
  "ds_read_b64_tr_b16 v[148:149], v161 offset:24576\n\t" \
  "v_mfma_f32_16x16x32_bf16 v[12:15], v[128:131], a[216:219], v[12:15]\n\t" \
  "v_exp_f32 v37, v37\n\t" \
  "v_mfma_f32_16x16x32_bf16 v[12:15], v[132:135], a[220:223], v[12:15]\n\t" \
  "ds_read_b64_tr_b16 v[150:151], v161 offset:28672\n\t" \
  "v_cvt_pk_bf16_f32 v58, v36, v37\n\t" \
  "v_mfma_f32_16x16x32_bf16 v[16:19], v[120:123], a[224:227], 0\n\t" \
  "s_waitcnt lgkmcnt(8)\n\t" \
  "v_exp_f32 v38, v38\n\t" \
  "v_mfma_f32_16x16x32_bf16 v[16:19], v[124:127], a[228:231], v[16:19]\n\t" \
  "ds_read_b64_tr_b16 v[236:237], v165 offset:8192\n\t" \
  "v_mfma_f32_16x16x32_bf16 v[16:19], v[128:131], a[232:235], v[16:19]\n\t" \
  "s_waitcnt lgkmcnt(5)\n\t" \
  "v_exp_f32 v39, v39\n\t" \
  "v_mfma_f32_16x16x32_bf16 v[16:19], v[132:135], a[236:239], v[16:19]\n\t" \
  "v_cvt_pk_bf16_f32 v59, v38, v39\n\t" \
  "v_mfma_f32_16x16x32_bf16 v[176:179], v[232:235], v[228:231], v[176:179]\n\t" \
  "ds_read_b64_tr_b16 v[238:239], v165 offset:12288\n\t" \
  "v_mfma_f32_16x16x32_bf16 a[80:83], v[136:139], v[80:83], a[80:83]\n\t" \
  "v_exp_f32 v0, v0\n\t" \
  "v_mfma_f32_16x16x32_bf16 a[80:83], v[140:143], v[100:103], a[80:83]\n\t" \
  "ds_read_b128 v[120:123], v152 offset:45056\n\t" \
  "v_mfma_f32_16x16x32_bf16 a[84:87], v[136:139], v[84:87], a[84:87]\n\t" \
  "v_exp_f32 v1, v1\n\t" \
  "v_mfma_f32_16x16x32_bf16 a[84:87], v[140:143], v[104:107], a[84:87]\n\t" \
  "ds_read_b128 v[124:127], v153 offset:45056\n\t" \
  "v_cvt_pk_bf16_f32 v60, v0, v1\n\t" \
  "v_mfma_f32_16x16x32_bf16 a[88:91], v[136:139], v[88:91], a[88:91]\n\t" \
  "s_waitcnt lgkmcnt(4)\n\t" \
  "v_exp_f32 v2, v2\n\t" \
  "v_mfma_f32_16x16x32_bf16 a[88:91], v[140:143], v[108:111], a[88:91]\n\t" \
  "ds_read_b128 v[128:131], v154 offset:45056\n\t" \
  "v_mfma_f32_16x16x32_bf16 a[92:95], v[136:139], v[92:95], a[92:95]\n\t" \
  "v_exp_f32 v3, v3\n\t" \
  "v_mfma_f32_16x16x32_bf16 a[92:95], v[140:143], v[112:115], a[92:95]\n\t" \
  "ds_read_b128 v[132:135], v155 offset:45056\n\t" \
  "v_cvt_pk_bf16_f32 v61, v2, v3\n\t" \
  "v_mfma_f32_16x16x32_bf16 a[96:99], v[136:139], v[96:99], a[96:99]\n\t" \
  "s_add_u32 m0, s74, 0\n\t" \
  "v_exp_f32 v4, v4\n\t" \
  "v_mfma_f32_16x16x32_bf16 a[96:99], v[140:143], v[116:119], a[96:99]\n\t" \
  "buffer_load_dwordx4 v240, %[srdg], s84 offen offset:0 lds\n\t" \
  "v_mfma_f32_16x16x32_bf16 a[100:103], v[144:147], v[80:83], a[100:103]\n\t" \
  "v_exp_f32 v5, v5\n\t" \
  "v_mfma_f32_16x16x32_bf16 a[100:103], v[148:151], v[100:103], a[100:103]\n\t" \
  "buffer_load_dwordx4 v241, %[srdg], s85 offen offset:1024 lds\n\t" \
  "v_mfma_f32_16x16x32_bf16 a[104:107], v[144:147], v[84:87], a[104:107]\n\t" \
  "v_exp_f32 v6, v6\n\t" \
  "v_mfma_f32_16x16x32_bf16 a[104:107], v[148:151], v[104:107], a[104:107]\n\t" \
  "buffer_load_dwordx4 v242, %[srdg], s86 offen offset:2048 lds\n\t" \
  "v_mfma_f32_16x16x32_bf16 a[108:111], v[144:147], v[88:91], a[108:111]\n\t" \
  "v_exp_f32 v7, v7\n\t" \
  "v_mfma_f32_16x16x32_bf16 a[108:111], v[148:151], v[108:111], a[108:111]\n\t" \
  "v_cvt_pk_bf16_f32 v64, v4, v5\n\t" \
  "v_cvt_pk_bf16_f32 v65, v6, v7\n\t" \
  "v_mfma_f32_16x16x32_bf16 a[112:115], v[144:147], v[92:95], a[112:115]\n\t" \
  "s_waitcnt lgkmcnt(4)\n\t" \
  "v_exp_f32 v8, v8\n\t" \
  "v_mfma_f32_16x16x32_bf16 a[112:115], v[148:151], v[112:115], a[112:115]\n\t" \
  "ds_read_b64_tr_b16 v[232:233], v166 offset:8192\n\t" \
  "v_xor_b32_dpp v240, v212, v247 row_newbcast:0 row_mask:0xf bank_mask:0xf\n\t" \
  "v_mfma_f32_16x16x32_bf16 a[116:119], v[144:147], v[96:99], a[116:119]\n\t" \
  "s_waitcnt lgkmcnt(1)\n\t" \
  "v_exp_f32 v9, v9\n\t" \
  "v_mfma_f32_16x16x32_bf16 a[116:119], v[148:151], v[116:119], a[116:119]\n\t" \
  "buffer_load_dwordx4 v243, %[srdg], s87 offen offset:3072 lds\n\t" \
  "v_mfma_f32_16x16x32_bf16 v[180:183], v[236:239], v[228:231], v[180:183]\n\t" \
  "ds_read_b64_tr_b16 v[234:235], v166 offset:12288\n\t" \
  "v_cvt_pk_bf16_f32 v68, v8, v9\n\t" \
  "v_mfma_f32_16x16x32_bf16 v[20:23], v[120:123], a[160:163], 0\n\t" \
  "v_exp_f32 v10, v10\n\t" \
  "v_mfma_f32_16x16x32_bf16 v[20:23], v[124:127], a[164:167], v[20:23]\n\t" \
  "ds_read_b64_tr_b16 v[136:137], v162 offset:16384\n\t" \
  "v_xor_b32_dpp v241, v213, v247 row_newbcast:0 row_mask:0xf bank_mask:0xf\n\t" \
  "v_mfma_f32_16x16x32_bf16 v[20:23], v[128:131], a[168:171], v[20:23]\n\t" \
  "v_exp_f32 v11, v11\n\t" \
  "v_mfma_f32_16x16x32_bf16 v[20:23], v[132:135], a[172:175], v[20:23]\n\t" \
  "ds_read_b64_tr_b16 v[138:139], v162 offset:20480\n\t" \
  "v_cvt_pk_bf16_f32 v69, v10, v11\n\t" \
  "v_mfma_f32_16x16x32_bf16 v[24:27], v[120:123], a[176:179], 0\n\t" \
  "v_exp_f32 v12, v12\n\t" \
  "v_mfma_f32_16x16x32_bf16 v[24:27], v[124:127], a[180:183], v[24:27]\n\t" \
  "ds_read_b64_tr_b16 v[140:141], v162 offset:24576\n\t" \
  "v_xor_b32_dpp v242, v214, v247 row_newbcast:0 row_mask:0xf bank_mask:0xf\n\t" \
  "v_mfma_f32_16x16x32_bf16 v[24:27], v[128:131], a[184:187], v[24:27]\n\t" \
  "v_exp_f32 v13, v13\n\t" \
  "v_mfma_f32_16x16x32_bf16 v[24:27], v[132:135], a[188:191], v[24:27]\n\t" \
  "ds_read_b64_tr_b16 v[142:143], v162 offset:28672\n\t" \
  "v_cvt_pk_bf16_f32 v72, v12, v13\n\t" \
  "v_mfma_f32_16x16x32_bf16 v[28:31], v[120:123], a[192:195], 0\n\t" \
  "v_exp_f32 v14, v14\n\t" \
  "v_mfma_f32_16x16x32_bf16 v[28:31], v[124:127], a[196:199], v[28:31]\n\t" \
  "ds_read_b64_tr_b16 v[144:145], v163 offset:16384\n\t" \
  "v_xor_b32_dpp v243, v215, v247 row_newbcast:0 row_mask:0xf bank_mask:0xf\n\t" \
  "v_mfma_f32_16x16x32_bf16 v[28:31], v[128:131], a[200:203], v[28:31]\n\t" \
  "v_exp_f32 v15, v15\n\t" \
  "v_mfma_f32_16x16x32_bf16 v[28:31], v[132:135], a[204:207], v[28:31]\n\t" \
  "ds_read_b64_tr_b16 v[146:147], v163 offset:20480\n\t" \
  "v_cvt_pk_bf16_f32 v73, v14, v15\n\t" \
  "v_mfma_f32_16x16x32_bf16 v[32:35], v[120:123], a[208:211], 0\n\t" \
  "v_exp_f32 v16, v16\n\t" \
  "v_mfma_f32_16x16x32_bf16 v[32:35], v[124:127], a[212:215], v[32:35]\n\t" \
  "ds_read_b64_tr_b16 v[148:149], v163 offset:24576\n\t" \
  "v_mfma_f32_16x16x32_bf16 v[32:35], v[128:131], a[216:219], v[32:35]\n\t" \
  "v_exp_f32 v17, v17\n\t" \
  "v_mfma_f32_16x16x32_bf16 v[32:35], v[132:135], a[220:223], v[32:35]\n\t" \
  "ds_read_b64_tr_b16 v[150:151], v163 offset:28672\n\t" \
  "v_cvt_pk_bf16_f32 v76, v16, v17\n\t" \
  "v_mfma_f32_16x16x32_bf16 v[36:39], v[120:123], a[224:227], 0\n\t" \
  "s_waitcnt lgkmcnt(8)\n\t" \
  "v_exp_f32 v18, v18\n\t" \
  "v_mfma_f32_16x16x32_bf16 v[36:39], v[124:127], a[228:231], v[36:39]\n\t" \
  "ds_read_b64_tr_b16 v[236:237], v167 offset:8192\n\t" \
  "v_mfma_f32_16x16x32_bf16 v[36:39], v[128:131], a[232:235], v[36:39]\n\t" \
  "s_waitcnt lgkmcnt(5)\n\t" \
  "v_exp_f32 v19, v19\n\t" \
  "v_mfma_f32_16x16x32_bf16 v[36:39], v[132:135], a[236:239], v[36:39]\n\t" \
  "v_cvt_pk_bf16_f32 v77, v18, v19\n\t" \
  "v_mfma_f32_16x16x32_bf16 v[184:187], v[232:235], v[228:231], v[184:187]\n\t" \
  "ds_read_b64_tr_b16 v[238:239], v167 offset:12288\n\t" \
  "v_mfma_f32_16x16x32_bf16 a[120:123], v[136:139], v[80:83], a[120:123]\n\t" \
  "s_add_u32 m0, s74, 4096\n\t" \
  "v_exp_f32 v20, v20\n\t" \
  "v_mfma_f32_16x16x32_bf16 a[120:123], v[140:143], v[100:103], a[120:123]\n\t" \
  "buffer_load_dwordx4 v240, %[srdg], s84 offen offset:0 lds\n\t" \
  "v_mfma_f32_16x16x32_bf16 a[124:127], v[136:139], v[84:87], a[124:127]\n\t" \
  "v_exp_f32 v21, v21\n\t" \
  "v_mfma_f32_16x16x32_bf16 a[124:127], v[140:143], v[104:107], a[124:127]\n\t" \
  "buffer_load_dwordx4 v241, %[srdg], s85 offen offset:1024 lds\n\t" \
  "v_mfma_f32_16x16x32_bf16 a[128:131], v[136:139], v[88:91], a[128:131]\n\t" \
  "s_waitcnt lgkmcnt(2)\n\t" \
  "v_exp_f32 v22, v22\n\t" \
  "v_mfma_f32_16x16x32_bf16 a[128:131], v[140:143], v[108:111], a[128:131]\n\t" \
  "buffer_load_dwordx4 v242, %[srdg], s86 offen offset:2048 lds\n\t" \
  "v_mfma_f32_16x16x32_bf16 a[132:135], v[136:139], v[92:95], a[132:135]\n\t" \
  "s_waitcnt vmcnt(7)\n\t" \
  "s_barrier\n\t" \
  "v_exp_f32 v23, v23\n\t" \
  "v_mfma_f32_16x16x32_bf16 a[132:135], v[140:143], v[112:115], a[132:135]\n\t" \
  "ds_read_b128 v[120:123], v152 offset:0\n\t" \
  "v_cvt_pk_bf16_f32 v62, v20, v21\n\t" \
  "v_mfma_f32_16x16x32_bf16 a[136:139], v[136:139], v[96:99], a[136:139]\n\t" \
  "v_exp_f32 v24, v24\n\t" \
  "v_mfma_f32_16x16x32_bf16 a[136:139], v[140:143], v[116:119], a[136:139]\n\t" \
  "ds_read_b128 v[124:127], v153 offset:0\n\t" \
  "v_cvt_pk_bf16_f32 v63, v22, v23\n\t" \
  "v_mfma_f32_16x16x32_bf16 a[140:143], v[144:147], v[80:83], a[140:143]\n\t" \
  "v_exp_f32 v25, v25\n\t" \
  "v_mfma_f32_16x16x32_bf16 a[140:143], v[148:151], v[100:103], a[140:143]\n\t" \
  "ds_read_b128 v[128:131], v154 offset:0\n\t" \
  "v_cvt_pk_bf16_f32 v66, v24, v25\n\t" \
  "v_mfma_f32_16x16x32_bf16 a[144:147], v[144:147], v[84:87], a[144:147]\n\t" \
  "v_exp_f32 v26, v26\n\t" \
  "v_mfma_f32_16x16x32_bf16 a[144:147], v[148:151], v[104:107], a[144:147]\n\t" \
  "ds_read_b128 v[132:135], v155 offset:0\n\t" \
  "v_mfma_f32_16x16x32_bf16 a[148:151], v[144:147], v[88:91], a[148:151]\n\t" \
  "v_exp_f32 v27, v27\n\t" \
  "v_mfma_f32_16x16x32_bf16 a[148:151], v[148:151], v[108:111], a[148:151]\n\t" \
  "v_cvt_pk_bf16_f32 v67, v26, v27\n\t" \
  "v_mfma_f32_16x16x32_bf16 a[152:155], v[144:147], v[92:95], a[152:155]\n\t" \
  "s_waitcnt lgkmcnt(4)\n\t" \
  "v_exp_f32 v28, v28\n\t" \
  "v_mfma_f32_16x16x32_bf16 a[152:155], v[148:151], v[112:115], a[152:155]\n\t" \
  "ds_read_b64_tr_b16 v[232:233], v168 offset:8192\n\t" \
  "v_mfma_f32_16x16x32_bf16 a[156:159], v[144:147], v[96:99], a[156:159]\n\t" \
  "s_waitcnt lgkmcnt(1)\n\t" \
  "v_exp_f32 v29, v29\n\t" \
  "v_mfma_f32_16x16x32_bf16 a[156:159], v[148:151], v[116:119], a[156:159]\n\t" \
  "buffer_load_dwordx4 v243, %[srdg], s87 offen offset:3072 lds\n\t" \
  "v_mfma_f32_16x16x32_bf16 v[188:191], v[236:239], v[228:231], v[188:191]\n\t" \
  "ds_read_b64_tr_b16 v[234:235], v168 offset:12288\n\t" \
  "v_cvt_pk_bf16_f32 v70, v28, v29\n\t" \
  "v_mfma_f32_16x16x32_bf16 v[0:3], v[120:123], a[160:163], 0\n\t" \
  "v_exp_f32 v30, v30\n\t" \
  "v_mfma_f32_16x16x32_bf16 v[0:3], v[124:127], a[164:167], v[0:3]\n\t" \
  "ds_read_b64_tr_b16 v[136:137], v156 offset:32768\n\t" \
  "v_mov_b32_dpp v244, v216 row_newbcast:1 row_mask:0xf bank_mask:0xf\n\t" \
  "v_mfma_f32_16x16x32_bf16 v[0:3], v[128:131], a[168:171], v[0:3]\n\t" \
  "v_exp_f32 v31, v31\n\t" \
  "v_mfma_f32_16x16x32_bf16 v[0:3], v[132:135], a[172:175], v[0:3]\n\t" \
  "ds_read_b64_tr_b16 v[138:139], v156 offset:36864\n\t" \
  "v_cvt_pk_bf16_f32 v71, v30, v31\n\t" \
  "v_mfma_f32_16x16x32_bf16 v[4:7], v[120:123], a[176:179], 0\n\t" \
  "v_exp_f32 v32, v32\n\t" \
  "v_mfma_f32_16x16x32_bf16 v[4:7], v[124:127], a[180:183], v[4:7]\n\t" \
  "ds_read_b64_tr_b16 v[140:141], v156 offset:40960\n\t" \
  "v_alignbit_b32 v244, v244, v244, v248\n\t" \
  "v_mfma_f32_16x16x32_bf16 v[4:7], v[128:131], a[184:187], v[4:7]\n\t" \
  "v_exp_f32 v33, v33\n\t" \
  "v_mfma_f32_16x16x32_bf16 v[4:7], v[132:135], a[188:191], v[4:7]\n\t" \
  "ds_read_b64_tr_b16 v[142:143], v156 offset:45056\n\t" \
  "v_cvt_pk_bf16_f32 v74, v32, v33\n\t" \
  "v_mfma_f32_16x16x32_bf16 v[8:11], v[120:123], a[192:195], 0\n\t" \
  "v_exp_f32 v34, v34\n\t" \
  "v_mfma_f32_16x16x32_bf16 v[8:11], v[124:127], a[196:199], v[8:11]\n\t" \
  "ds_read_b64_tr_b16 v[144:145], v157 offset:32768\n\t" \
  "v_and_b32 v224, v244, v249\n\t" \
  "v_mfma_f32_16x16x32_bf16 v[8:11], v[128:131], a[200:203], v[8:11]\n\t" \
  "v_exp_f32 v35, v35\n\t" \
  "v_mfma_f32_16x16x32_bf16 v[8:11], v[132:135], a[204:207], v[8:11]\n\t" \
  "ds_read_b64_tr_b16 v[146:147], v157 offset:36864\n\t" \
  "v_cvt_pk_bf16_f32 v75, v34, v35\n\t" \
  "v_mfma_f32_16x16x32_bf16 v[12:15], v[120:123], a[208:211], 0\n\t" \
  "v_exp_f32 v36, v36\n\t" \
  "v_mfma_f32_16x16x32_bf16 v[12:15], v[124:127], a[212:215], v[12:15]\n\t" \
  "ds_read_b64_tr_b16 v[148:149], v157 offset:40960\n\t" \
  "v_mov_b32_dpp v245, v217 row_newbcast:1 row_mask:0xf bank_mask:0xf\n\t" \
  "v_mfma_f32_16x16x32_bf16 v[12:15], v[128:131], a[216:219], v[12:15]\n\t" \
  "v_exp_f32 v37, v37\n\t" \
  "v_mfma_f32_16x16x32_bf16 v[12:15], v[132:135], a[220:223], v[12:15]\n\t" \
  "ds_read_b64_tr_b16 v[150:151], v157 offset:45056\n\t" \
  "v_cvt_pk_bf16_f32 v78, v36, v37\n\t" \
  "v_mfma_f32_16x16x32_bf16 v[16:19], v[120:123], a[224:227], 0\n\t" \
  "s_waitcnt lgkmcnt(8)\n\t" \
  "v_exp_f32 v38, v38\n\t" \
  "v_mfma_f32_16x16x32_bf16 v[16:19], v[124:127], a[228:231], v[16:19]\n\t" \
  "ds_read_b64_tr_b16 v[236:237], v169 offset:8192\n\t" \
  "v_alignbit_b32 v245, v245, v245, v248\n\t" \
  "v_mfma_f32_16x16x32_bf16 v[16:19], v[128:131], a[232:235], v[16:19]\n\t" \
  "s_waitcnt lgkmcnt(5)\n\t" \
  "v_exp_f32 v39, v39\n\t" \
  "v_mfma_f32_16x16x32_bf16 v[16:19], v[132:135], a[236:239], v[16:19]\n\t" \
  "v_cvt_pk_bf16_f32 v79, v38, v39\n\t" \
  "v_and_b32 v225, v245, v249\n\t" \
  "v_mfma_f32_16x16x32_bf16 v[192:195], v[232:235], v[228:231], v[192:195]\n\t" \
  "ds_read_b64_tr_b16 v[238:239], v169 offset:12288\n\t" \
  "v_mfma_f32_16x16x32_bf16 a[0:3], v[136:139], v[40:43], a[0:3]\n\t" \
  "v_exp_f32 v0, v0\n\t" \
  "v_mfma_f32_16x16x32_bf16 a[0:3], v[140:143], v[60:63], a[0:3]\n\t" \
  "ds_read_b128 v[120:123], v152 offset:4096\n\t" \
  "v_mfma_f32_16x16x32_bf16 a[4:7], v[136:139], v[44:47], a[4:7]\n\t" \
  "v_exp_f32 v1, v1\n\t" \
  "v_mfma_f32_16x16x32_bf16 a[4:7], v[140:143], v[64:67], a[4:7]\n\t" \
  "ds_read_b128 v[124:127], v153 offset:4096\n\t" \
  "v_cvt_pk_bf16_f32 v80, v0, v1\n\t" \
  "v_mfma_f32_16x16x32_bf16 a[8:11], v[136:139], v[48:51], a[8:11]\n\t" \
  "s_waitcnt lgkmcnt(4)\n\t" \
  "v_exp_f32 v2, v2\n\t" \
  "v_mfma_f32_16x16x32_bf16 a[8:11], v[140:143], v[68:71], a[8:11]\n\t" \
  "ds_read_b128 v[128:131], v154 offset:4096\n\t" \
  "v_mfma_f32_16x16x32_bf16 a[12:15], v[136:139], v[52:55], a[12:15]\n\t" \
  "s_add_u32 %[soff], %[soff], 12288\n\t" \
  "v_exp_f32 v3, v3\n\t" \
  "v_mfma_f32_16x16x32_bf16 a[12:15], v[140:143], v[72:75], a[12:15]\n\t" \
  "ds_read_b128 v[132:135], v155 offset:4096\n\t" \
  "v_cvt_pk_bf16_f32 v81, v2, v3\n\t" \
  "v_mfma_f32_16x16x32_bf16 a[16:19], v[136:139], v[56:59], a[16:19]\n\t" \
  "s_add_u32 m0, s72, 16384\n\t" \
  "v_exp_f32 v4, v4\n\t" \
  "v_mfma_f32_16x16x32_bf16 a[16:19], v[140:143], v[76:79], a[16:19]\n\t" \
  "buffer_load_dwordx4 v172, %[srd], %[soff] offen offset:0 lds\n\t" \
  "v_mfma_f32_16x16x32_bf16 a[20:23], v[144:147], v[40:43], a[20:23]\n\t" \
  "v_exp_f32 v5, v5\n\t" \
  "v_mfma_f32_16x16x32_bf16 a[20:23], v[148:151], v[60:63], a[20:23]\n\t" \
  "buffer_load_dwordx4 v172, %[srd], %[soff] offen offset:1024 lds\n\t" \
  "v_mfma_f32_16x16x32_bf16 a[24:27], v[144:147], v[44:47], a[24:27]\n\t" \
  "v_exp_f32 v6, v6\n\t" \
  "v_mfma_f32_16x16x32_bf16 a[24:27], v[148:151], v[64:67], a[24:27]\n\t" \
  "buffer_load_dwordx4 v172, %[srd], %[soff] offen offset:2048 lds\n\t" \
  "v_mfma_f32_16x16x32_bf16 a[28:31], v[144:147], v[48:51], a[28:31]\n\t" \
  "v_exp_f32 v7, v7\n\t" \
  "v_mfma_f32_16x16x32_bf16 a[28:31], v[148:151], v[68:71], a[28:31]\n\t" \
  "v_cvt_pk_bf16_f32 v84, v4, v5\n\t" \
  "v_cvt_pk_bf16_f32 v85, v6, v7\n\t" \
  "v_mfma_f32_16x16x32_bf16 a[32:35], v[144:147], v[52:55], a[32:35]\n\t" \
  "s_waitcnt lgkmcnt(4)\n\t" \
  "v_exp_f32 v8, v8\n\t" \
  "v_mfma_f32_16x16x32_bf16 a[32:35], v[148:151], v[72:75], a[32:35]\n\t" \
  "ds_read_b64_tr_b16 v[232:233], v170 offset:8192\n\t" \
  "v_mfma_f32_16x16x32_bf16 a[36:39], v[144:147], v[56:59], a[36:39]\n\t" \
  "s_waitcnt lgkmcnt(1)\n\t" \
  "v_exp_f32 v9, v9\n\t" \
  "v_mfma_f32_16x16x32_bf16 a[36:39], v[148:151], v[76:79], a[36:39]\n\t" \
  "buffer_load_dwordx4 v172, %[srd], %[soff] offen offset:3072 lds\n\t" \
  "v_mfma_f32_16x16x32_bf16 v[196:199], v[236:239], v[228:231], v[196:199]\n\t" \
  "ds_read_b64_tr_b16 v[234:235], v170 offset:12288\n\t" \
  "v_cvt_pk_bf16_f32 v88, v8, v9\n\t" \
  "v_mfma_f32_16x16x32_bf16 v[20:23], v[120:123], a[160:163], 0\n\t" \
  "v_exp_f32 v10, v10\n\t" \
  "v_mfma_f32_16x16x32_bf16 v[20:23], v[124:127], a[164:167], v[20:23]\n\t" \
  "ds_read_b64_tr_b16 v[136:137], v158 offset:32768\n\t" \
  "v_mov_b32_dpp v244, v218 row_newbcast:1 row_mask:0xf bank_mask:0xf\n\t" \
  "v_mfma_f32_16x16x32_bf16 v[20:23], v[128:131], a[168:171], v[20:23]\n\t" \
  "v_exp_f32 v11, v11\n\t" \
  "v_mfma_f32_16x16x32_bf16 v[20:23], v[132:135], a[172:175], v[20:23]\n\t" \
  "ds_read_b64_tr_b16 v[138:139], v158 offset:36864\n\t" \
  "v_cvt_pk_bf16_f32 v89, v10, v11\n\t" \
  "v_mfma_f32_16x16x32_bf16 v[24:27], v[120:123], a[176:179], 0\n\t" \
  "v_exp_f32 v12, v12\n\t" \
  "v_mfma_f32_16x16x32_bf16 v[24:27], v[124:127], a[180:183], v[24:27]\n\t" \
  "ds_read_b64_tr_b16 v[140:141], v158 offset:40960\n\t" \
  "v_alignbit_b32 v244, v244, v244, v248\n\t" \
  "v_mfma_f32_16x16x32_bf16 v[24:27], v[128:131], a[184:187], v[24:27]\n\t" \
  "v_exp_f32 v13, v13\n\t" \
  "v_mfma_f32_16x16x32_bf16 v[24:27], v[132:135], a[188:191], v[24:27]\n\t" \
  "ds_read_b64_tr_b16 v[142:143], v158 offset:45056\n\t" \
  "v_cvt_pk_bf16_f32 v92, v12, v13\n\t" \
  "v_mfma_f32_16x16x32_bf16 v[28:31], v[120:123], a[192:195], 0\n\t" \
  "v_exp_f32 v14, v14\n\t" \
  "v_mfma_f32_16x16x32_bf16 v[28:31], v[124:127], a[196:199], v[28:31]\n\t" \
  "ds_read_b64_tr_b16 v[144:145], v159 offset:32768\n\t" \
  "v_and_b32 v226, v244, v249\n\t" \
  "v_mfma_f32_16x16x32_bf16 v[28:31], v[128:131], a[200:203], v[28:31]\n\t" \
  "v_exp_f32 v15, v15\n\t" \
  "v_mfma_f32_16x16x32_bf16 v[28:31], v[132:135], a[204:207], v[28:31]\n\t" \
  "ds_read_b64_tr_b16 v[146:147], v159 offset:36864\n\t" \
  "v_cvt_pk_bf16_f32 v93, v14, v15\n\t" \
  "v_mfma_f32_16x16x32_bf16 v[32:35], v[120:123], a[208:211], 0\n\t" \
  "v_exp_f32 v16, v16\n\t" \
  "v_mfma_f32_16x16x32_bf16 v[32:35], v[124:127], a[212:215], v[32:35]\n\t" \
  "ds_read_b64_tr_b16 v[148:149], v159 offset:40960\n\t" \
  "v_mov_b32_dpp v245, v219 row_newbcast:1 row_mask:0xf bank_mask:0xf\n\t" \
  "v_mfma_f32_16x16x32_bf16 v[32:35], v[128:131], a[216:219], v[32:35]\n\t" \
  "v_exp_f32 v17, v17\n\t" \
  "v_mfma_f32_16x16x32_bf16 v[32:35], v[132:135], a[220:223], v[32:35]\n\t" \
  "ds_read_b64_tr_b16 v[150:151], v159 offset:45056\n\t" \
  "v_cvt_pk_bf16_f32 v96, v16, v17\n\t" \
  "v_mfma_f32_16x16x32_bf16 v[36:39], v[120:123], a[224:227], 0\n\t" \
  "s_waitcnt lgkmcnt(8)\n\t" \
  "v_exp_f32 v18, v18\n\t" \
  "v_mfma_f32_16x16x32_bf16 v[36:39], v[124:127], a[228:231], v[36:39]\n\t" \
  "ds_read_b64_tr_b16 v[236:237], v171 offset:8192\n\t" \
  "v_alignbit_b32 v245, v245, v245, v248\n\t" \
  "v_mfma_f32_16x16x32_bf16 v[36:39], v[128:131], a[232:235], v[36:39]\n\t" \
  "s_waitcnt lgkmcnt(5)\n\t" \
  "v_exp_f32 v19, v19\n\t" \
  "v_mfma_f32_16x16x32_bf16 v[36:39], v[132:135], a[236:239], v[36:39]\n\t" \
  "v_cvt_pk_bf16_f32 v97, v18, v19\n\t" \
  "v_and_b32 v227, v245, v249\n\t" \
  "v_mfma_f32_16x16x32_bf16 v[200:203], v[232:235], v[228:231], v[200:203]\n\t" \
  "ds_read_b64_tr_b16 v[238:239], v171 offset:12288\n\t" \
  "v_mfma_f32_16x16x32_bf16 a[40:43], v[136:139], v[40:43], a[40:43]\n\t" \
  "v_exp_f32 v20, v20\n\t" \
  "v_mfma_f32_16x16x32_bf16 a[40:43], v[140:143], v[60:63], a[40:43]\n\t" \
  "ds_read_b128 v[120:123], v152 offset:8192\n\t" \
  "v_xor_b32_dpp v240, v208, v247 row_newbcast:1 row_mask:0xf bank_mask:0xf\n\t" \
  "v_mfma_f32_16x16x32_bf16 a[44:47], v[136:139], v[44:47], a[44:47]\n\t" \
  "v_exp_f32 v21, v21\n\t" \
  "v_mfma_f32_16x16x32_bf16 a[44:47], v[140:143], v[64:67], a[44:47]\n\t" \
  "ds_read_b128 v[124:127], v153 offset:8192\n\t" \
  "v_cvt_pk_bf16_f32 v82, v20, v21\n\t" \
  "v_mfma_f32_16x16x32_bf16 a[48:51], v[136:139], v[48:51], a[48:51]\n\t" \
  "s_waitcnt lgkmcnt(4)\n\t" \
  "v_exp_f32 v22, v22\n\t" \
  "v_mfma_f32_16x16x32_bf16 a[48:51], v[140:143], v[68:71], a[48:51]\n\t" \
  "ds_read_b128 v[128:131], v154 offset:8192\n\t" \
  "v_xor_b32_dpp v241, v209, v247 row_newbcast:1 row_mask:0xf bank_mask:0xf\n\t" \
  "v_mfma_f32_16x16x32_bf16 a[52:55], v[136:139], v[52:55], a[52:55]\n\t" \
  "s_add_u32 %[soff], %[soff], 4096\n\t" \
  "v_exp_f32 v23, v23\n\t" \
  "v_mfma_f32_16x16x32_bf16 a[52:55], v[140:143], v[72:75], a[52:55]\n\t" \
  "ds_read_b128 v[132:135], v155 offset:8192\n\t" \
  "v_cvt_pk_bf16_f32 v83, v22, v23\n\t" \
  "v_mfma_f32_16x16x32_bf16 a[56:59], v[136:139], v[56:59], a[56:59]\n\t" \
  "s_add_u32 m0, s72, 20480\n\t" \
  "v_exp_f32 v24, v24\n\t" \
  "v_mfma_f32_16x16x32_bf16 a[56:59], v[140:143], v[76:79], a[56:59]\n\t" \
  "buffer_load_dwordx4 v172, %[srd], %[soff] offen offset:0 lds\n\t" \
  "v_mfma_f32_16x16x32_bf16 a[60:63], v[144:147], v[40:43], a[60:63]\n\t" \
  "v_exp_f32 v25, v25\n\t" \
  "v_mfma_f32_16x16x32_bf16 a[60:63], v[148:151], v[60:63], a[60:63]\n\t" \
  "buffer_load_dwordx4 v172, %[srd], %[soff] offen offset:1024 lds\n\t" \
  "v_mfma_f32_16x16x32_bf16 a[64:67], v[144:147], v[44:47], a[64:67]\n\t" \
  "v_exp_f32 v26, v26\n\t" \
  "v_mfma_f32_16x16x32_bf16 a[64:67], v[148:151], v[64:67], a[64:67]\n\t" \
  "buffer_load_dwordx4 v172, %[srd], %[soff] offen offset:2048 lds\n\t" \
  "v_mfma_f32_16x16x32_bf16 a[68:71], v[144:147], v[48:51], a[68:71]\n\t" \
  "s_waitcnt vmcnt(7)\n\t" \
  "v_exp_f32 v27, v27\n\t" \
  "v_mfma_f32_16x16x32_bf16 a[68:71], v[148:151], v[68:71], a[68:71]\n\t" \
  "v_cvt_pk_bf16_f32 v86, v24, v25\n\t" \
  "v_cvt_pk_bf16_f32 v87, v26, v27\n\t" \
  "v_mfma_f32_16x16x32_bf16 a[72:75], v[144:147], v[52:55], a[72:75]\n\t" \
  "s_waitcnt lgkmcnt(4)\n\t" \
  "v_exp_f32 v28, v28\n\t" \
  "v_mfma_f32_16x16x32_bf16 a[72:75], v[148:151], v[72:75], a[72:75]\n\t" \
  "ds_read_b64_tr_b16 v[232:233], v164 offset:0\n\t" \
  "v_xor_b32_dpp v242, v210, v247 row_newbcast:1 row_mask:0xf bank_mask:0xf\n\t" \
  "v_mfma_f32_16x16x32_bf16 a[76:79], v[144:147], v[56:59], a[76:79]\n\t" \
  "s_waitcnt lgkmcnt(1)\n\t" \
  "v_exp_f32 v29, v29\n\t" \
  "v_mfma_f32_16x16x32_bf16 a[76:79], v[148:151], v[76:79], a[76:79]\n\t" \
  "buffer_load_dwordx4 v172, %[srd], %[soff] offen offset:3072 lds\n\t" \
  "v_mfma_f32_16x16x32_bf16 v[204:207], v[236:239], v[228:231], v[204:207]\n\t" \
  "ds_read_b64_tr_b16 v[234:235], v164 offset:4096\n\t" \
  "v_cvt_pk_bf16_f32 v90, v28, v29\n\t" \
  "v_mfma_f32_16x16x32_bf16 v[0:3], v[120:123], a[160:163], 0\n\t" \
  "v_exp_f32 v30, v30\n\t" \
  "v_mfma_f32_16x16x32_bf16 v[0:3], v[124:127], a[164:167], v[0:3]\n\t" \
  "ds_read_b64_tr_b16 v[136:137], v160 offset:32768\n\t" \
  "v_xor_b32_dpp v243, v211, v247 row_newbcast:1 row_mask:0xf bank_mask:0xf\n\t" \
  "v_mfma_f32_16x16x32_bf16 v[0:3], v[128:131], a[168:171], v[0:3]\n\t" \
  "v_exp_f32 v31, v31\n\t" \
  "v_mfma_f32_16x16x32_bf16 v[0:3], v[132:135], a[172:175], v[0:3]\n\t" \
  "ds_read_b64_tr_b16 v[138:139], v160 offset:36864\n\t" \
  "v_cvt_pk_bf16_f32 v91, v30, v31\n\t" \
  "v_mfma_f32_16x16x32_bf16 v[4:7], v[120:123], a[176:179], 0\n\t" \
  "v_exp_f32 v32, v32\n\t" \
  "v_mfma_f32_16x16x32_bf16 v[4:7], v[124:127], a[180:183], v[4:7]\n\t" \
  "ds_read_b64_tr_b16 v[140:141], v160 offset:40960\n\t" \
  "v_mfma_f32_16x16x32_bf16 v[4:7], v[128:131], a[184:187], v[4:7]\n\t" \
  "v_exp_f32 v33, v33\n\t" \
  "v_mfma_f32_16x16x32_bf16 v[4:7], v[132:135], a[188:191], v[4:7]\n\t" \
  "ds_read_b64_tr_b16 v[142:143], v160 offset:45056\n\t" \
  "v_cvt_pk_bf16_f32 v94, v32, v33\n\t" \
  "v_mfma_f32_16x16x32_bf16 v[8:11], v[120:123], a[192:195], 0\n\t" \
  "v_exp_f32 v34, v34\n\t" \
  "v_mfma_f32_16x16x32_bf16 v[8:11], v[124:127], a[196:199], v[8:11]\n\t" \
  "ds_read_b64_tr_b16 v[144:145], v161 offset:32768\n\t" \
  "v_mfma_f32_16x16x32_bf16 v[8:11], v[128:131], a[200:203], v[8:11]\n\t" \
  "v_exp_f32 v35, v35\n\t" \
  "v_mfma_f32_16x16x32_bf16 v[8:11], v[132:135], a[204:207], v[8:11]\n\t" \
  "ds_read_b64_tr_b16 v[146:147], v161 offset:36864\n\t" \
  "v_cvt_pk_bf16_f32 v95, v34, v35\n\t" \
  "v_mfma_f32_16x16x32_bf16 v[12:15], v[120:123], a[208:211], 0\n\t" \
  "v_exp_f32 v36, v36\n\t" \
  "v_mfma_f32_16x16x32_bf16 v[12:15], v[124:127], a[212:215], v[12:15]\n\t" \
  "ds_read_b64_tr_b16 v[148:149], v161 offset:40960\n\t" \
  "v_mfma_f32_16x16x32_bf16 v[12:15], v[128:131], a[216:219], v[12:15]\n\t" \
  "v_exp_f32 v37, v37\n\t" \
  "v_mfma_f32_16x16x32_bf16 v[12:15], v[132:135], a[220:223], v[12:15]\n\t" \
  "ds_read_b64_tr_b16 v[150:151], v161 offset:45056\n\t" \
  "v_cvt_pk_bf16_f32 v98, v36, v37\n\t" \
  "v_mfma_f32_16x16x32_bf16 v[16:19], v[120:123], a[224:227], 0\n\t" \
  "s_waitcnt lgkmcnt(8)\n\t" \
  "v_exp_f32 v38, v38\n\t" \
  "v_mfma_f32_16x16x32_bf16 v[16:19], v[124:127], a[228:231], v[16:19]\n\t" \
  "ds_read_b64_tr_b16 v[236:237], v165 offset:0\n\t" \
  "v_mfma_f32_16x16x32_bf16 v[16:19], v[128:131], a[232:235], v[16:19]\n\t" \
  "s_waitcnt lgkmcnt(5)\n\t" \
  "v_exp_f32 v39, v39\n\t" \
  "v_mfma_f32_16x16x32_bf16 v[16:19], v[132:135], a[236:239], v[16:19]\n\t" \
  "v_cvt_pk_bf16_f32 v99, v38, v39\n\t" \
  "v_mfma_f32_16x16x32_bf16 v[176:179], v[232:235], v[220:223], v[176:179]\n\t" \
  "ds_read_b64_tr_b16 v[238:239], v165 offset:4096\n\t" \
  "v_mfma_f32_16x16x32_bf16 a[80:83], v[136:139], v[40:43], a[80:83]\n\t" \
  "v_exp_f32 v0, v0\n\t" \
  "v_mfma_f32_16x16x32_bf16 a[80:83], v[140:143], v[60:63], a[80:83]\n\t" \
  "ds_read_b128 v[120:123], v152 offset:12288\n\t" \
  "v_mfma_f32_16x16x32_bf16 a[84:87], v[136:139], v[44:47], a[84:87]\n\t" \
  "v_exp_f32 v1, v1\n\t" \
  "v_mfma_f32_16x16x32_bf16 a[84:87], v[140:143], v[64:67], a[84:87]\n\t" \
  "ds_read_b128 v[124:127], v153 offset:12288\n\t" \
  "v_cvt_pk_bf16_f32 v100, v0, v1\n\t" \
  "v_mfma_f32_16x16x32_bf16 a[88:91], v[136:139], v[48:51], a[88:91]\n\t" \
  "s_waitcnt lgkmcnt(4)\n\t" \
  "v_exp_f32 v2, v2\n\t" \
  "v_mfma_f32_16x16x32_bf16 a[88:91], v[140:143], v[68:71], a[88:91]\n\t" \
  "ds_read_b128 v[128:131], v154 offset:12288\n\t" \
  "v_mfma_f32_16x16x32_bf16 a[92:95], v[136:139], v[52:55], a[92:95]\n\t" \
  "v_exp_f32 v3, v3\n\t" \
  "v_mfma_f32_16x16x32_bf16 a[92:95], v[140:143], v[72:75], a[92:95]\n\t" \
  "ds_read_b128 v[132:135], v155 offset:12288\n\t" \
  "v_cvt_pk_bf16_f32 v101, v2, v3\n\t" \
  "v_mfma_f32_16x16x32_bf16 a[96:99], v[136:139], v[56:59], a[96:99]\n\t" \
  "s_add_u32 m0, s74, 8192\n\t" \
  "v_exp_f32 v4, v4\n\t" \
  "v_mfma_f32_16x16x32_bf16 a[96:99], v[140:143], v[76:79], a[96:99]\n\t" \
  "buffer_load_dwordx4 v240, %[srdg], s84 offen offset:0 lds\n\t" \
  "v_mfma_f32_16x16x32_bf16 a[100:103], v[144:147], v[40:43], a[100:103]\n\t" \
  "v_exp_f32 v5, v5\n\t" \
  "v_mfma_f32_16x16x32_bf16 a[100:103], v[148:151], v[60:63], a[100:103]\n\t" \
  "buffer_load_dwordx4 v241, %[srdg], s85 offen offset:1024 lds\n\t" \
  "v_mfma_f32_16x16x32_bf16 a[104:107], v[144:147], v[44:47], a[104:107]\n\t" \
  "v_exp_f32 v6, v6\n\t" \
  "v_mfma_f32_16x16x32_bf16 a[104:107], v[148:151], v[64:67], a[104:107]\n\t" \
  "buffer_load_dwordx4 v242, %[srdg], s86 offen offset:2048 lds\n\t" \
  "v_mfma_f32_16x16x32_bf16 a[108:111], v[144:147], v[48:51], a[108:111]\n\t" \
  "v_exp_f32 v7, v7\n\t" \
  "v_mfma_f32_16x16x32_bf16 a[108:111], v[148:151], v[68:71], a[108:111]\n\t" \
  "v_cvt_pk_bf16_f32 v104, v4, v5\n\t" \
  "v_cvt_pk_bf16_f32 v105, v6, v7\n\t" \
  "v_mfma_f32_16x16x32_bf16 a[112:115], v[144:147], v[52:55], a[112:115]\n\t" \
  "s_waitcnt lgkmcnt(4)\n\t" \
  "v_exp_f32 v8, v8\n\t" \
  "v_mfma_f32_16x16x32_bf16 a[112:115], v[148:151], v[72:75], a[112:115]\n\t" \
  "ds_read_b64_tr_b16 v[232:233], v166 offset:0\n\t" \
  "v_xor_b32_dpp v240, v212, v247 row_newbcast:1 row_mask:0xf bank_mask:0xf\n\t" \
  "v_mfma_f32_16x16x32_bf16 a[116:119], v[144:147], v[56:59], a[116:119]\n\t" \
  "s_waitcnt lgkmcnt(1)\n\t" \
  "v_exp_f32 v9, v9\n\t" \
  "v_mfma_f32_16x16x32_bf16 a[116:119], v[148:151], v[76:79], a[116:119]\n\t" \
  "buffer_load_dwordx4 v243, %[srdg], s87 offen offset:3072 lds\n\t" \
  "v_mfma_f32_16x16x32_bf16 v[180:183], v[236:239], v[220:223], v[180:183]\n\t" \
  "ds_read_b64_tr_b16 v[234:235], v166 offset:4096\n\t" \
  "v_cvt_pk_bf16_f32 v108, v8, v9\n\t" \
  "v_mfma_f32_16x16x32_bf16 v[20:23], v[120:123], a[160:163], 0\n\t" \
  "v_exp_f32 v10, v10\n\t" \
  "v_mfma_f32_16x16x32_bf16 v[20:23], v[124:127], a[164:167], v[20:23]\n\t" \
  "ds_read_b64_tr_b16 v[136:137], v162 offset:32768\n\t" \
  "v_xor_b32_dpp v241, v213, v247 row_newbcast:1 row_mask:0xf bank_mask:0xf\n\t" \
  "v_mfma_f32_16x16x32_bf16 v[20:23], v[128:131], a[168:171], v[20:23]\n\t" \
  "v_exp_f32 v11, v11\n\t" \
  "v_mfma_f32_16x16x32_bf16 v[20:23], v[132:135], a[172:175], v[20:23]\n\t" \
  "ds_read_b64_tr_b16 v[138:139], v162 offset:36864\n\t" \
  "v_cvt_pk_bf16_f32 v109, v10, v11\n\t" \
  "v_mfma_f32_16x16x32_bf16 v[24:27], v[120:123], a[176:179], 0\n\t" \
  "v_exp_f32 v12, v12\n\t" \
  "v_mfma_f32_16x16x32_bf16 v[24:27], v[124:127], a[180:183], v[24:27]\n\t" \
  "ds_read_b64_tr_b16 v[140:141], v162 offset:40960\n\t" \
  "v_xor_b32_dpp v242, v214, v247 row_newbcast:1 row_mask:0xf bank_mask:0xf\n\t" \
  "v_mfma_f32_16x16x32_bf16 v[24:27], v[128:131], a[184:187], v[24:27]\n\t" \
  "v_exp_f32 v13, v13\n\t" \
  "v_mfma_f32_16x16x32_bf16 v[24:27], v[132:135], a[188:191], v[24:27]\n\t" \
  "ds_read_b64_tr_b16 v[142:143], v162 offset:45056\n\t" \
  "v_cvt_pk_bf16_f32 v112, v12, v13\n\t" \
  "v_mfma_f32_16x16x32_bf16 v[28:31], v[120:123], a[192:195], 0\n\t" \
  "v_exp_f32 v14, v14\n\t" \
  "v_mfma_f32_16x16x32_bf16 v[28:31], v[124:127], a[196:199], v[28:31]\n\t" \
  "ds_read_b64_tr_b16 v[144:145], v163 offset:32768\n\t" \
  "v_xor_b32_dpp v243, v215, v247 row_newbcast:1 row_mask:0xf bank_mask:0xf\n\t" \
  "v_mfma_f32_16x16x32_bf16 v[28:31], v[128:131], a[200:203], v[28:31]\n\t" \
  "v_exp_f32 v15, v15\n\t" \
  "v_mfma_f32_16x16x32_bf16 v[28:31], v[132:135], a[204:207], v[28:31]\n\t" \
  "ds_read_b64_tr_b16 v[146:147], v163 offset:36864\n\t" \
  "v_cvt_pk_bf16_f32 v113, v14, v15\n\t" \
  "v_mfma_f32_16x16x32_bf16 v[32:35], v[120:123], a[208:211], 0\n\t" \
  "v_exp_f32 v16, v16\n\t" \
  "v_mfma_f32_16x16x32_bf16 v[32:35], v[124:127], a[212:215], v[32:35]\n\t" \
  "ds_read_b64_tr_b16 v[148:149], v163 offset:40960\n\t" \
  "v_mfma_f32_16x16x32_bf16 v[32:35], v[128:131], a[216:219], v[32:35]\n\t" \
  "v_exp_f32 v17, v17\n\t" \
  "v_mfma_f32_16x16x32_bf16 v[32:35], v[132:135], a[220:223], v[32:35]\n\t" \
  "ds_read_b64_tr_b16 v[150:151], v163 offset:45056\n\t" \
  "v_cvt_pk_bf16_f32 v116, v16, v17\n\t" \
  "v_mfma_f32_16x16x32_bf16 v[36:39], v[120:123], a[224:227], 0\n\t" \
  "s_waitcnt lgkmcnt(8)\n\t" \
  "v_exp_f32 v18, v18\n\t" \
  "v_mfma_f32_16x16x32_bf16 v[36:39], v[124:127], a[228:231], v[36:39]\n\t" \
  "ds_read_b64_tr_b16 v[236:237], v167 offset:0\n\t" \
  "v_mfma_f32_16x16x32_bf16 v[36:39], v[128:131], a[232:235], v[36:39]\n\t" \
  "s_waitcnt lgkmcnt(5)\n\t" \
  "v_exp_f32 v19, v19\n\t" \
  "v_mfma_f32_16x16x32_bf16 v[36:39], v[132:135], a[236:239], v[36:39]\n\t" \
  "v_cvt_pk_bf16_f32 v117, v18, v19\n\t" \
  "v_mfma_f32_16x16x32_bf16 v[184:187], v[232:235], v[220:223], v[184:187]\n\t" \
  "ds_read_b64_tr_b16 v[238:239], v167 offset:4096\n\t" \
  "v_mfma_f32_16x16x32_bf16 a[120:123], v[136:139], v[40:43], a[120:123]\n\t" \
  "s_add_u32 m0, s74, 12288\n\t" \
  "v_exp_f32 v20, v20\n\t" \
  "v_mfma_f32_16x16x32_bf16 a[120:123], v[140:143], v[60:63], a[120:123]\n\t" \
  "buffer_load_dwordx4 v240, %[srdg], s84 offen offset:0 lds\n\t" \
  "v_mfma_f32_16x16x32_bf16 a[124:127], v[136:139], v[44:47], a[124:127]\n\t" \
  "v_exp_f32 v21, v21\n\t" \
  "v_mfma_f32_16x16x32_bf16 a[124:127], v[140:143], v[64:67], a[124:127]\n\t" \
  "buffer_load_dwordx4 v241, %[srdg], s85 offen offset:1024 lds\n\t" \
  "v_mfma_f32_16x16x32_bf16 a[128:131], v[136:139], v[48:51], a[128:131]\n\t" \
  "s_waitcnt lgkmcnt(2)\n\t" \
  "v_exp_f32 v22, v22\n\t" \
  "v_mfma_f32_16x16x32_bf16 a[128:131], v[140:143], v[68:71], a[128:131]\n\t" \
  "buffer_load_dwordx4 v242, %[srdg], s86 offen offset:2048 lds\n\t" \
  "v_mfma_f32_16x16x32_bf16 a[132:135], v[136:139], v[52:55], a[132:135]\n\t" \
  "s_waitcnt vmcnt(7)\n\t" \
  "s_barrier\n\t" \
  "v_exp_f32 v23, v23\n\t" \
  "v_mfma_f32_16x16x32_bf16 a[132:135], v[140:143], v[72:75], a[132:135]\n\t" \
  "ds_read_b128 v[120:123], v152 offset:16384\n\t" \
  "v_cvt_pk_bf16_f32 v102, v20, v21\n\t" \
  "v_mfma_f32_16x16x32_bf16 a[136:139], v[136:139], v[56:59], a[136:139]\n\t" \
  "v_exp_f32 v24, v24\n\t" \
  "v_mfma_f32_16x16x32_bf16 a[136:139], v[140:143], v[76:79], a[136:139]\n\t" \
  "ds_read_b128 v[124:127], v153 offset:16384\n\t" \
  "v_cvt_pk_bf16_f32 v103, v22, v23\n\t" \
  "v_mfma_f32_16x16x32_bf16 a[140:143], v[144:147], v[40:43], a[140:143]\n\t" \
  "v_exp_f32 v25, v25\n\t" \
  "v_mfma_f32_16x16x32_bf16 a[140:143], v[148:151], v[60:63], a[140:143]\n\t" \
  "ds_read_b128 v[128:131], v154 offset:16384\n\t" \
  "v_cvt_pk_bf16_f32 v106, v24, v25\n\t" \
  "v_mfma_f32_16x16x32_bf16 a[144:147], v[144:147], v[44:47], a[144:147]\n\t" \
  "v_exp_f32 v26, v26\n\t" \
  "v_mfma_f32_16x16x32_bf16 a[144:147], v[148:151], v[64:67], a[144:147]\n\t" \
  "ds_read_b128 v[132:135], v155 offset:16384\n\t" \
  "v_mfma_f32_16x16x32_bf16 a[148:151], v[144:147], v[48:51], a[148:151]\n\t" \
  "v_exp_f32 v27, v27\n\t" \
  "v_mfma_f32_16x16x32_bf16 a[148:151], v[148:151], v[68:71], a[148:151]\n\t" \
  "v_cvt_pk_bf16_f32 v107, v26, v27\n\t" \
  "v_mfma_f32_16x16x32_bf16 a[152:155], v[144:147], v[52:55], a[152:155]\n\t" \
  "s_waitcnt lgkmcnt(4)\n\t" \
  "v_exp_f32 v28, v28\n\t" \
  "v_mfma_f32_16x16x32_bf16 a[152:155], v[148:151], v[72:75], a[152:155]\n\t" \
  "ds_read_b64_tr_b16 v[232:233], v168 offset:0\n\t" \
  "v_mfma_f32_16x16x32_bf16 a[156:159], v[144:147], v[56:59], a[156:159]\n\t" \
  "s_waitcnt lgkmcnt(1)\n\t" \
  "v_exp_f32 v29, v29\n\t" \
  "v_mfma_f32_16x16x32_bf16 a[156:159], v[148:151], v[76:79], a[156:159]\n\t" \
  "buffer_load_dwordx4 v243, %[srdg], s87 offen offset:3072 lds\n\t" \
  "v_mfma_f32_16x16x32_bf16 v[188:191], v[236:239], v[220:223], v[188:191]\n\t" \
  "ds_read_b64_tr_b16 v[234:235], v168 offset:4096\n\t" \
  "v_cvt_pk_bf16_f32 v110, v28, v29\n\t" \
  "v_mfma_f32_16x16x32_bf16 v[0:3], v[120:123], a[160:163], 0\n\t" \
  "v_exp_f32 v30, v30\n\t" \
  "v_mfma_f32_16x16x32_bf16 v[0:3], v[124:127], a[164:167], v[0:3]\n\t" \
  "ds_read_b64_tr_b16 v[136:137], v156 offset:0\n\t" \
  "v_mov_b32_dpp v244, v216 row_newbcast:2 row_mask:0xf bank_mask:0xf\n\t" \
  "v_mfma_f32_16x16x32_bf16 v[0:3], v[128:131], a[168:171], v[0:3]\n\t" \
  "v_exp_f32 v31, v31\n\t" \
  "v_mfma_f32_16x16x32_bf16 v[0:3], v[132:135], a[172:175], v[0:3]\n\t" \
  "ds_read_b64_tr_b16 v[138:139], v156 offset:4096\n\t" \
  "v_cvt_pk_bf16_f32 v111, v30, v31\n\t" \
  "v_mfma_f32_16x16x32_bf16 v[4:7], v[120:123], a[176:179], 0\n\t" \
  "v_exp_f32 v32, v32\n\t" \
  "v_mfma_f32_16x16x32_bf16 v[4:7], v[124:127], a[180:183], v[4:7]\n\t" \
  "ds_read_b64_tr_b16 v[140:141], v156 offset:8192\n\t" \
  "v_alignbit_b32 v244, v244, v244, v248\n\t" \
  "v_mfma_f32_16x16x32_bf16 v[4:7], v[128:131], a[184:187], v[4:7]\n\t" \
  "v_exp_f32 v33, v33\n\t" \
  "v_mfma_f32_16x16x32_bf16 v[4:7], v[132:135], a[188:191], v[4:7]\n\t" \
  "ds_read_b64_tr_b16 v[142:143], v156 offset:12288\n\t" \
  "v_cvt_pk_bf16_f32 v114, v32, v33\n\t" \
  "v_mfma_f32_16x16x32_bf16 v[8:11], v[120:123], a[192:195], 0\n\t" \
  "v_exp_f32 v34, v34\n\t" \
  "v_mfma_f32_16x16x32_bf16 v[8:11], v[124:127], a[196:199], v[8:11]\n\t" \
  "ds_read_b64_tr_b16 v[144:145], v157 offset:0\n\t" \
  "v_and_b32 v228, v244, v249\n\t" \
  "v_mfma_f32_16x16x32_bf16 v[8:11], v[128:131], a[200:203], v[8:11]\n\t" \
  "v_exp_f32 v35, v35\n\t" \
  "v_mfma_f32_16x16x32_bf16 v[8:11], v[132:135], a[204:207], v[8:11]\n\t" \
  "ds_read_b64_tr_b16 v[146:147], v157 offset:4096\n\t" \
  "v_cvt_pk_bf16_f32 v115, v34, v35\n\t" \
  "v_mfma_f32_16x16x32_bf16 v[12:15], v[120:123], a[208:211], 0\n\t" \
  "v_exp_f32 v36, v36\n\t" \
  "v_mfma_f32_16x16x32_bf16 v[12:15], v[124:127], a[212:215], v[12:15]\n\t" \
  "ds_read_b64_tr_b16 v[148:149], v157 offset:8192\n\t" \
  "v_mov_b32_dpp v245, v217 row_newbcast:2 row_mask:0xf bank_mask:0xf\n\t" \
  "v_mfma_f32_16x16x32_bf16 v[12:15], v[128:131], a[216:219], v[12:15]\n\t" \
  "v_exp_f32 v37, v37\n\t" \
  "v_mfma_f32_16x16x32_bf16 v[12:15], v[132:135], a[220:223], v[12:15]\n\t" \
  "ds_read_b64_tr_b16 v[150:151], v157 offset:12288\n\t" \
  "v_cvt_pk_bf16_f32 v118, v36, v37\n\t" \
  "v_mfma_f32_16x16x32_bf16 v[16:19], v[120:123], a[224:227], 0\n\t" \
  "s_waitcnt lgkmcnt(8)\n\t" \
  "v_exp_f32 v38, v38\n\t" \
  "v_mfma_f32_16x16x32_bf16 v[16:19], v[124:127], a[228:231], v[16:19]\n\t" \
  "ds_read_b64_tr_b16 v[236:237], v169 offset:0\n\t" \
  "v_alignbit_b32 v245, v245, v245, v248\n\t" \
  "v_mfma_f32_16x16x32_bf16 v[16:19], v[128:131], a[232:235], v[16:19]\n\t" \
  "s_waitcnt lgkmcnt(5)\n\t" \
  "v_exp_f32 v39, v39\n\t" \
  "v_mfma_f32_16x16x32_bf16 v[16:19], v[132:135], a[236:239], v[16:19]\n\t" \
  "v_cvt_pk_bf16_f32 v119, v38, v39\n\t" \
  "v_and_b32 v229, v245, v249\n\t" \
  "v_mfma_f32_16x16x32_bf16 v[192:195], v[232:235], v[220:223], v[192:195]\n\t" \
  "ds_read_b64_tr_b16 v[238:239], v169 offset:4096\n\t" \
  "v_mfma_f32_16x16x32_bf16 a[0:3], v[136:139], v[80:83], a[0:3]\n\t" \
  "v_exp_f32 v0, v0\n\t" \
  "v_mfma_f32_16x16x32_bf16 a[0:3], v[140:143], v[100:103], a[0:3]\n\t" \
  "ds_read_b128 v[120:123], v152 offset:20480\n\t" \
  "v_mfma_f32_16x16x32_bf16 a[4:7], v[136:139], v[84:87], a[4:7]\n\t" \
  "v_exp_f32 v1, v1\n\t" \
  "v_mfma_f32_16x16x32_bf16 a[4:7], v[140:143], v[104:107], a[4:7]\n\t" \
  "ds_read_b128 v[124:127], v153 offset:20480\n\t" \
  "v_cvt_pk_bf16_f32 v40, v0, v1\n\t" \
  "v_mfma_f32_16x16x32_bf16 a[8:11], v[136:139], v[88:91], a[8:11]\n\t" \
  "s_waitcnt lgkmcnt(4)\n\t" \
  "v_exp_f32 v2, v2\n\t" \
  "v_mfma_f32_16x16x32_bf16 a[8:11], v[140:143], v[108:111], a[8:11]\n\t" \
  "ds_read_b128 v[128:131], v154 offset:20480\n\t" \
  "v_mfma_f32_16x16x32_bf16 a[12:15], v[136:139], v[92:95], a[12:15]\n\t" \
  "s_add_u32 %[soff], %[soff], 12288\n\t" \
  "v_exp_f32 v3, v3\n\t" \
  "v_mfma_f32_16x16x32_bf16 a[12:15], v[140:143], v[112:115], a[12:15]\n\t" \
  "ds_read_b128 v[132:135], v155 offset:20480\n\t" \
  "v_cvt_pk_bf16_f32 v41, v2, v3\n\t" \
  "v_mfma_f32_16x16x32_bf16 a[16:19], v[136:139], v[96:99], a[16:19]\n\t" \
  "s_add_u32 m0, s72, 32768\n\t" \
  "v_exp_f32 v4, v4\n\t" \
  "v_mfma_f32_16x16x32_bf16 a[16:19], v[140:143], v[116:119], a[16:19]\n\t" \
  "buffer_load_dwordx4 v172, %[srd], %[soff] offen offset:0 lds\n\t" \
  "v_mfma_f32_16x16x32_bf16 a[20:23], v[144:147], v[80:83], a[20:23]\n\t" \
  "v_exp_f32 v5, v5\n\t" \
  "v_mfma_f32_16x16x32_bf16 a[20:23], v[148:151], v[100:103], a[20:23]\n\t" \
  "buffer_load_dwordx4 v172, %[srd], %[soff] offen offset:1024 lds\n\t" \
  "v_mfma_f32_16x16x32_bf16 a[24:27], v[144:147], v[84:87], a[24:27]\n\t" \
  "v_exp_f32 v6, v6\n\t" \
  "v_mfma_f32_16x16x32_bf16 a[24:27], v[148:151], v[104:107], a[24:27]\n\t" \
  "buffer_load_dwordx4 v172, %[srd], %[soff] offen offset:2048 lds\n\t" \
  "v_mfma_f32_16x16x32_bf16 a[28:31], v[144:147], v[88:91], a[28:31]\n\t" \
  "v_exp_f32 v7, v7\n\t" \
  "v_mfma_f32_16x16x32_bf16 a[28:31], v[148:151], v[108:111], a[28:31]\n\t" \
  "v_cvt_pk_bf16_f32 v44, v4, v5\n\t" \
  "v_cvt_pk_bf16_f32 v45, v6, v7\n\t" \
  "v_mfma_f32_16x16x32_bf16 a[32:35], v[144:147], v[92:95], a[32:35]\n\t" \
  "s_waitcnt lgkmcnt(4)\n\t" \
  "v_exp_f32 v8, v8\n\t" \
  "v_mfma_f32_16x16x32_bf16 a[32:35], v[148:151], v[112:115], a[32:35]\n\t" \
  "ds_read_b64_tr_b16 v[232:233], v170 offset:0\n\t" \
  "v_mfma_f32_16x16x32_bf16 a[36:39], v[144:147], v[96:99], a[36:39]\n\t" \
  "s_waitcnt lgkmcnt(1)\n\t" \
  "v_exp_f32 v9, v9\n\t" \
  "v_mfma_f32_16x16x32_bf16 a[36:39], v[148:151], v[116:119], a[36:39]\n\t" \
  "buffer_load_dwordx4 v172, %[srd], %[soff] offen offset:3072 lds\n\t" \
  "v_mfma_f32_16x16x32_bf16 v[196:199], v[236:239], v[220:223], v[196:199]\n\t" \
  "ds_read_b64_tr_b16 v[234:235], v170 offset:4096\n\t" \
  "v_cvt_pk_bf16_f32 v48, v8, v9\n\t" \
  "v_mfma_f32_16x16x32_bf16 v[20:23], v[120:123], a[160:163], 0\n\t" \
  "v_exp_f32 v10, v10\n\t" \
  "v_mfma_f32_16x16x32_bf16 v[20:23], v[124:127], a[164:167], v[20:23]\n\t" \
  "ds_read_b64_tr_b16 v[136:137], v158 offset:0\n\t" \
  "v_mov_b32_dpp v244, v218 row_newbcast:2 row_mask:0xf bank_mask:0xf\n\t" \
  "v_mfma_f32_16x16x32_bf16 v[20:23], v[128:131], a[168:171], v[20:23]\n\t" \
  "v_exp_f32 v11, v11\n\t" \
  "v_mfma_f32_16x16x32_bf16 v[20:23], v[132:135], a[172:175], v[20:23]\n\t" \
  "ds_read_b64_tr_b16 v[138:139], v158 offset:4096\n\t" \
  "v_cvt_pk_bf16_f32 v49, v10, v11\n\t" \
  "v_mfma_f32_16x16x32_bf16 v[24:27], v[120:123], a[176:179], 0\n\t" \
  "v_exp_f32 v12, v12\n\t" \
  "v_mfma_f32_16x16x32_bf16 v[24:27], v[124:127], a[180:183], v[24:27]\n\t" \
  "ds_read_b64_tr_b16 v[140:141], v158 offset:8192\n\t" \
  "v_alignbit_b32 v244, v244, v244, v248\n\t" \
  "v_mfma_f32_16x16x32_bf16 v[24:27], v[128:131], a[184:187], v[24:27]\n\t" \
  "v_exp_f32 v13, v13\n\t" \
  "v_mfma_f32_16x16x32_bf16 v[24:27], v[132:135], a[188:191], v[24:27]\n\t" \
  "ds_read_b64_tr_b16 v[142:143], v158 offset:12288\n\t" \
  "v_cvt_pk_bf16_f32 v52, v12, v13\n\t" \
  "v_mfma_f32_16x16x32_bf16 v[28:31], v[120:123], a[192:195], 0\n\t" \
  "v_exp_f32 v14, v14\n\t" \
  "v_mfma_f32_16x16x32_bf16 v[28:31], v[124:127], a[196:199], v[28:31]\n\t" \
  "ds_read_b64_tr_b16 v[144:145], v159 offset:0\n\t" \
  "v_and_b32 v230, v244, v249\n\t" \
  "v_mfma_f32_16x16x32_bf16 v[28:31], v[128:131], a[200:203], v[28:31]\n\t" \
  "v_exp_f32 v15, v15\n\t" \
  "v_mfma_f32_16x16x32_bf16 v[28:31], v[132:135], a[204:207], v[28:31]\n\t" \
  "ds_read_b64_tr_b16 v[146:147], v159 offset:4096\n\t" \
  "v_cvt_pk_bf16_f32 v53, v14, v15\n\t" \
  "v_mfma_f32_16x16x32_bf16 v[32:35], v[120:123], a[208:211], 0\n\t" \
  "v_exp_f32 v16, v16\n\t" \
  "v_mfma_f32_16x16x32_bf16 v[32:35], v[124:127], a[212:215], v[32:35]\n\t" \
  "ds_read_b64_tr_b16 v[148:149], v159 offset:8192\n\t" \
  "v_mov_b32_dpp v245, v219 row_newbcast:2 row_mask:0xf bank_mask:0xf\n\t" \
  "v_mfma_f32_16x16x32_bf16 v[32:35], v[128:131], a[216:219], v[32:35]\n\t" \
  "v_exp_f32 v17, v17\n\t" \
  "v_mfma_f32_16x16x32_bf16 v[32:35], v[132:135], a[220:223], v[32:35]\n\t" \
  "ds_read_b64_tr_b16 v[150:151], v159 offset:12288\n\t" \
  "v_cvt_pk_bf16_f32 v56, v16, v17\n\t" \
  "v_mfma_f32_16x16x32_bf16 v[36:39], v[120:123], a[224:227], 0\n\t" \
  "s_waitcnt lgkmcnt(8)\n\t" \
  "v_exp_f32 v18, v18\n\t" \
  "v_mfma_f32_16x16x32_bf16 v[36:39], v[124:127], a[228:231], v[36:39]\n\t" \
  "ds_read_b64_tr_b16 v[236:237], v171 offset:0\n\t" \
  "v_alignbit_b32 v245, v245, v245, v248\n\t" \
  "v_mfma_f32_16x16x32_bf16 v[36:39], v[128:131], a[232:235], v[36:39]\n\t" \
  "s_waitcnt lgkmcnt(5)\n\t" \
  "v_exp_f32 v19, v19\n\t" \
  "v_mfma_f32_16x16x32_bf16 v[36:39], v[132:135], a[236:239], v[36:39]\n\t" \
  "v_cvt_pk_bf16_f32 v57, v18, v19\n\t" \
  "v_and_b32 v231, v245, v249\n\t" \
  "v_mfma_f32_16x16x32_bf16 v[200:203], v[232:235], v[220:223], v[200:203]\n\t" \
  "ds_read_b64_tr_b16 v[238:239], v171 offset:4096\n\t" \
  "v_mfma_f32_16x16x32_bf16 a[40:43], v[136:139], v[80:83], a[40:43]\n\t" \
  "v_exp_f32 v20, v20\n\t" \
  "v_mfma_f32_16x16x32_bf16 a[40:43], v[140:143], v[100:103], a[40:43]\n\t" \
  "ds_read_b128 v[120:123], v152 offset:24576\n\t" \
  "v_xor_b32_dpp v240, v208, v247 row_newbcast:2 row_mask:0xf bank_mask:0xf\n\t" \
  "v_mfma_f32_16x16x32_bf16 a[44:47], v[136:139], v[84:87], a[44:47]\n\t" \
  "v_exp_f32 v21, v21\n\t" \
  "v_mfma_f32_16x16x32_bf16 a[44:47], v[140:143], v[104:107], a[44:47]\n\t" \
  "ds_read_b128 v[124:127], v153 offset:24576\n\t" \
  "v_cvt_pk_bf16_f32 v42, v20, v21\n\t" \
  "v_mfma_f32_16x16x32_bf16 a[48:51], v[136:139], v[88:91], a[48:51]\n\t" \
  "s_waitcnt lgkmcnt(4)\n\t" \
  "v_exp_f32 v22, v22\n\t" \
  "v_mfma_f32_16x16x32_bf16 a[48:51], v[140:143], v[108:111], a[48:51]\n\t" \
  "ds_read_b128 v[128:131], v154 offset:24576\n\t" \
  "v_xor_b32_dpp v241, v209, v247 row_newbcast:2 row_mask:0xf bank_mask:0xf\n\t" \
  "v_mfma_f32_16x16x32_bf16 a[52:55], v[136:139], v[92:95], a[52:55]\n\t" \
  "s_add_u32 %[soff], %[soff], 4096\n\t" \
  "v_exp_f32 v23, v23\n\t" \
  "v_mfma_f32_16x16x32_bf16 a[52:55], v[140:143], v[112:115], a[52:55]\n\t" \
  "ds_read_b128 v[132:135], v155 offset:24576\n\t" \
  "v_cvt_pk_bf16_f32 v43, v22, v23\n\t" \
  "v_mfma_f32_16x16x32_bf16 a[56:59], v[136:139], v[96:99], a[56:59]\n\t" \
  "s_add_u32 m0, s72, 36864\n\t" \
  "v_exp_f32 v24, v24\n\t" \
  "v_mfma_f32_16x16x32_bf16 a[56:59], v[140:143], v[116:119], a[56:59]\n\t" \
  "buffer_load_dwordx4 v172, %[srd], %[soff] offen offset:0 lds\n\t" \
  "v_mfma_f32_16x16x32_bf16 a[60:63], v[144:147], v[80:83], a[60:63]\n\t" \
  "v_exp_f32 v25, v25\n\t" \
  "v_mfma_f32_16x16x32_bf16 a[60:63], v[148:151], v[100:103], a[60:63]\n\t" \
  "buffer_load_dwordx4 v172, %[srd], %[soff] offen offset:1024 lds\n\t" \
  "v_mfma_f32_16x16x32_bf16 a[64:67], v[144:147], v[84:87], a[64:67]\n\t" \
  "v_exp_f32 v26, v26\n\t" \
  "v_mfma_f32_16x16x32_bf16 a[64:67], v[148:151], v[104:107], a[64:67]\n\t" \
  "buffer_load_dwordx4 v172, %[srd], %[soff] offen offset:2048 lds\n\t" \
  "v_mfma_f32_16x16x32_bf16 a[68:71], v[144:147], v[88:91], a[68:71]\n\t" \
  "s_waitcnt vmcnt(7)\n\t" \
  "v_exp_f32 v27, v27\n\t" \
  "v_mfma_f32_16x16x32_bf16 a[68:71], v[148:151], v[108:111], a[68:71]\n\t" \
  "v_cvt_pk_bf16_f32 v46, v24, v25\n\t" \
  "v_cvt_pk_bf16_f32 v47, v26, v27\n\t" \
  "v_mfma_f32_16x16x32_bf16 a[72:75], v[144:147], v[92:95], a[72:75]\n\t" \
  "s_waitcnt lgkmcnt(4)\n\t" \
  "v_exp_f32 v28, v28\n\t" \
  "v_mfma_f32_16x16x32_bf16 a[72:75], v[148:151], v[112:115], a[72:75]\n\t" \
  "ds_read_b64_tr_b16 v[232:233], v164 offset:8192\n\t" \
  "v_xor_b32_dpp v242, v210, v247 row_newbcast:2 row_mask:0xf bank_mask:0xf\n\t" \
  "v_mfma_f32_16x16x32_bf16 a[76:79], v[144:147], v[96:99], a[76:79]\n\t" \
  "s_waitcnt lgkmcnt(1)\n\t" \
  "v_exp_f32 v29, v29\n\t" \
  "v_mfma_f32_16x16x32_bf16 a[76:79], v[148:151], v[116:119], a[76:79]\n\t" \
  "buffer_load_dwordx4 v172, %[srd], %[soff] offen offset:3072 lds\n\t" \
  "v_mfma_f32_16x16x32_bf16 v[204:207], v[236:239], v[220:223], v[204:207]\n\t" \
  "ds_read_b64_tr_b16 v[234:235], v164 offset:12288\n\t" \
  "v_cvt_pk_bf16_f32 v50, v28, v29\n\t" \
  "v_mfma_f32_16x16x32_bf16 v[0:3], v[120:123], a[160:163], 0\n\t" \
  "v_exp_f32 v30, v30\n\t" \
  "v_mfma_f32_16x16x32_bf16 v[0:3], v[124:127], a[164:167], v[0:3]\n\t" \
  "ds_read_b64_tr_b16 v[136:137], v160 offset:0\n\t" \
  "v_xor_b32_dpp v243, v211, v247 row_newbcast:2 row_mask:0xf bank_mask:0xf\n\t" \
  "v_mfma_f32_16x16x32_bf16 v[0:3], v[128:131], a[168:171], v[0:3]\n\t" \
  "v_exp_f32 v31, v31\n\t" \
  "v_mfma_f32_16x16x32_bf16 v[0:3], v[132:135], a[172:175], v[0:3]\n\t" \
  "ds_read_b64_tr_b16 v[138:139], v160 offset:4096\n\t" \
  "v_cvt_pk_bf16_f32 v51, v30, v31\n\t" \
  "v_mfma_f32_16x16x32_bf16 v[4:7], v[120:123], a[176:179], 0\n\t" \
  "v_exp_f32 v32, v32\n\t" \
  "v_mfma_f32_16x16x32_bf16 v[4:7], v[124:127], a[180:183], v[4:7]\n\t" \
  "ds_read_b64_tr_b16 v[140:141], v160 offset:8192\n\t" \
  "v_mfma_f32_16x16x32_bf16 v[4:7], v[128:131], a[184:187], v[4:7]\n\t" \
  "v_exp_f32 v33, v33\n\t" \
  "v_mfma_f32_16x16x32_bf16 v[4:7], v[132:135], a[188:191], v[4:7]\n\t" \
  "ds_read_b64_tr_b16 v[142:143], v160 offset:12288\n\t" \
  "v_cvt_pk_bf16_f32 v54, v32, v33\n\t" \
  "v_mfma_f32_16x16x32_bf16 v[8:11], v[120:123], a[192:195], 0\n\t" \
  "v_exp_f32 v34, v34\n\t" \
  "v_mfma_f32_16x16x32_bf16 v[8:11], v[124:127], a[196:199], v[8:11]\n\t" \
  "ds_read_b64_tr_b16 v[144:145], v161 offset:0\n\t" \
  "v_mfma_f32_16x16x32_bf16 v[8:11], v[128:131], a[200:203], v[8:11]\n\t" \
  "v_exp_f32 v35, v35\n\t" \
  "v_mfma_f32_16x16x32_bf16 v[8:11], v[132:135], a[204:207], v[8:11]\n\t" \
  "ds_read_b64_tr_b16 v[146:147], v161 offset:4096\n\t" \
  "v_cvt_pk_bf16_f32 v55, v34, v35\n\t" \
  "v_mfma_f32_16x16x32_bf16 v[12:15], v[120:123], a[208:211], 0\n\t" \
  "v_exp_f32 v36, v36\n\t" \
  "v_mfma_f32_16x16x32_bf16 v[12:15], v[124:127], a[212:215], v[12:15]\n\t" \
  "ds_read_b64_tr_b16 v[148:149], v161 offset:8192\n\t" \
  "v_mfma_f32_16x16x32_bf16 v[12:15], v[128:131], a[216:219], v[12:15]\n\t" \
  "v_exp_f32 v37, v37\n\t" \
  "v_mfma_f32_16x16x32_bf16 v[12:15], v[132:135], a[220:223], v[12:15]\n\t" \
  "ds_read_b64_tr_b16 v[150:151], v161 offset:12288\n\t" \
  "v_cvt_pk_bf16_f32 v58, v36, v37\n\t" \
  "v_mfma_f32_16x16x32_bf16 v[16:19], v[120:123], a[224:227], 0\n\t" \
  "s_waitcnt lgkmcnt(8)\n\t" \
  "v_exp_f32 v38, v38\n\t" \
  "v_mfma_f32_16x16x32_bf16 v[16:19], v[124:127], a[228:231], v[16:19]\n\t" \
  "ds_read_b64_tr_b16 v[236:237], v165 offset:8192\n\t" \
  "v_mfma_f32_16x16x32_bf16 v[16:19], v[128:131], a[232:235], v[16:19]\n\t" \
  "s_waitcnt lgkmcnt(5)\n\t" \
  "v_exp_f32 v39, v39\n\t" \
  "v_mfma_f32_16x16x32_bf16 v[16:19], v[132:135], a[236:239], v[16:19]\n\t" \
  "v_cvt_pk_bf16_f32 v59, v38, v39\n\t" \
  "v_mfma_f32_16x16x32_bf16 v[176:179], v[232:235], v[224:227], v[176:179]\n\t" \
  "ds_read_b64_tr_b16 v[238:239], v165 offset:12288\n\t" \
  "v_mfma_f32_16x16x32_bf16 a[80:83], v[136:139], v[80:83], a[80:83]\n\t" \
  "v_exp_f32 v0, v0\n\t" \
  "v_mfma_f32_16x16x32_bf16 a[80:83], v[140:143], v[100:103], a[80:83]\n\t" \
  "ds_read_b128 v[120:123], v152 offset:28672\n\t" \
  "v_mfma_f32_16x16x32_bf16 a[84:87], v[136:139], v[84:87], a[84:87]\n\t" \
  "v_exp_f32 v1, v1\n\t" \
  "v_mfma_f32_16x16x32_bf16 a[84:87], v[140:143], v[104:107], a[84:87]\n\t" \
  "ds_read_b128 v[124:127], v153 offset:28672\n\t" \
  "v_cvt_pk_bf16_f32 v60, v0, v1\n\t" \
  "v_mfma_f32_16x16x32_bf16 a[88:91], v[136:139], v[88:91], a[88:91]\n\t" \
  "s_waitcnt lgkmcnt(4)\n\t" \
  "v_exp_f32 v2, v2\n\t" \
  "v_mfma_f32_16x16x32_bf16 a[88:91], v[140:143], v[108:111], a[88:91]\n\t" \
  "ds_read_b128 v[128:131], v154 offset:28672\n\t" \
  "v_mfma_f32_16x16x32_bf16 a[92:95], v[136:139], v[92:95], a[92:95]\n\t" \
  "v_exp_f32 v3, v3\n\t" \
  "v_mfma_f32_16x16x32_bf16 a[92:95], v[140:143], v[112:115], a[92:95]\n\t" \
  "ds_read_b128 v[132:135], v155 offset:28672\n\t" \
  "v_cvt_pk_bf16_f32 v61, v2, v3\n\t" \
  "v_mfma_f32_16x16x32_bf16 a[96:99], v[136:139], v[96:99], a[96:99]\n\t" \
  "s_add_u32 m0, s74, 0\n\t" \
  "v_exp_f32 v4, v4\n\t" \
  "v_mfma_f32_16x16x32_bf16 a[96:99], v[140:143], v[116:119], a[96:99]\n\t" \
  "buffer_load_dwordx4 v240, %[srdg], s84 offen offset:0 lds\n\t" \
  "v_mfma_f32_16x16x32_bf16 a[100:103], v[144:147], v[80:83], a[100:103]\n\t" \
  "v_exp_f32 v5, v5\n\t" \
  "v_mfma_f32_16x16x32_bf16 a[100:103], v[148:151], v[100:103], a[100:103]\n\t" \
  "buffer_load_dwordx4 v241, %[srdg], s85 offen offset:1024 lds\n\t" \
  "v_mfma_f32_16x16x32_bf16 a[104:107], v[144:147], v[84:87], a[104:107]\n\t" \
  "v_exp_f32 v6, v6\n\t" \
  "v_mfma_f32_16x16x32_bf16 a[104:107], v[148:151], v[104:107], a[104:107]\n\t" \
  "buffer_load_dwordx4 v242, %[srdg], s86 offen offset:2048 lds\n\t" \
  "v_mfma_f32_16x16x32_bf16 a[108:111], v[144:147], v[88:91], a[108:111]\n\t" \
  "v_exp_f32 v7, v7\n\t" \
  "v_mfma_f32_16x16x32_bf16 a[108:111], v[148:151], v[108:111], a[108:111]\n\t" \
  "v_cvt_pk_bf16_f32 v64, v4, v5\n\t" \
  "v_cvt_pk_bf16_f32 v65, v6, v7\n\t" \
  "v_mfma_f32_16x16x32_bf16 a[112:115], v[144:147], v[92:95], a[112:115]\n\t" \
  "s_waitcnt lgkmcnt(4)\n\t" \
  "v_exp_f32 v8, v8\n\t" \
  "v_mfma_f32_16x16x32_bf16 a[112:115], v[148:151], v[112:115], a[112:115]\n\t" \
  "ds_read_b64_tr_b16 v[232:233], v166 offset:8192\n\t" \
  "v_xor_b32_dpp v240, v212, v247 row_newbcast:2 row_mask:0xf bank_mask:0xf\n\t" \
  "v_mfma_f32_16x16x32_bf16 a[116:119], v[144:147], v[96:99], a[116:119]\n\t" \
  "s_waitcnt lgkmcnt(1)\n\t" \
  "v_exp_f32 v9, v9\n\t" \
  "v_mfma_f32_16x16x32_bf16 a[116:119], v[148:151], v[116:119], a[116:119]\n\t" \
  "buffer_load_dwordx4 v243, %[srdg], s87 offen offset:3072 lds\n\t" \
  "v_mfma_f32_16x16x32_bf16 v[180:183], v[236:239], v[224:227], v[180:183]\n\t" \
  "ds_read_b64_tr_b16 v[234:235], v166 offset:12288\n\t" \
  "v_cvt_pk_bf16_f32 v68, v8, v9\n\t" \
  "v_mfma_f32_16x16x32_bf16 v[20:23], v[120:123], a[160:163], 0\n\t" \
  "v_exp_f32 v10, v10\n\t" \
  "v_mfma_f32_16x16x32_bf16 v[20:23], v[124:127], a[164:167], v[20:23]\n\t" \
  "ds_read_b64_tr_b16 v[136:137], v162 offset:0\n\t" \
  "v_xor_b32_dpp v241, v213, v247 row_newbcast:2 row_mask:0xf bank_mask:0xf\n\t" \
  "v_mfma_f32_16x16x32_bf16 v[20:23], v[128:131], a[168:171], v[20:23]\n\t" \
  "v_exp_f32 v11, v11\n\t" \
  "v_mfma_f32_16x16x32_bf16 v[20:23], v[132:135], a[172:175], v[20:23]\n\t" \
  "ds_read_b64_tr_b16 v[138:139], v162 offset:4096\n\t" \
  "v_cvt_pk_bf16_f32 v69, v10, v11\n\t" \
  "v_mfma_f32_16x16x32_bf16 v[24:27], v[120:123], a[176:179], 0\n\t" \
  "v_exp_f32 v12, v12\n\t" \
  "v_mfma_f32_16x16x32_bf16 v[24:27], v[124:127], a[180:183], v[24:27]\n\t" \
  "ds_read_b64_tr_b16 v[140:141], v162 offset:8192\n\t" \
  "v_xor_b32_dpp v242, v214, v247 row_newbcast:2 row_mask:0xf bank_mask:0xf\n\t" \
  "v_mfma_f32_16x16x32_bf16 v[24:27], v[128:131], a[184:187], v[24:27]\n\t" \
  "v_exp_f32 v13, v13\n\t" \
  "v_mfma_f32_16x16x32_bf16 v[24:27], v[132:135], a[188:191], v[24:27]\n\t" \
  "ds_read_b64_tr_b16 v[142:143], v162 offset:12288\n\t" \
  "v_cvt_pk_bf16_f32 v72, v12, v13\n\t" \
  "v_mfma_f32_16x16x32_bf16 v[28:31], v[120:123], a[192:195], 0\n\t" \
  "v_exp_f32 v14, v14\n\t" \
  "v_mfma_f32_16x16x32_bf16 v[28:31], v[124:127], a[196:199], v[28:31]\n\t" \
  "ds_read_b64_tr_b16 v[144:145], v163 offset:0\n\t" \
  "v_xor_b32_dpp v243, v215, v247 row_newbcast:2 row_mask:0xf bank_mask:0xf\n\t" \
  "v_mfma_f32_16x16x32_bf16 v[28:31], v[128:131], a[200:203], v[28:31]\n\t" \
  "v_exp_f32 v15, v15\n\t" \
  "v_mfma_f32_16x16x32_bf16 v[28:31], v[132:135], a[204:207], v[28:31]\n\t" \
  "ds_read_b64_tr_b16 v[146:147], v163 offset:4096\n\t" \
  "v_cvt_pk_bf16_f32 v73, v14, v15\n\t" \
  "v_mfma_f32_16x16x32_bf16 v[32:35], v[120:123], a[208:211], 0\n\t" \
  "v_exp_f32 v16, v16\n\t" \
  "v_mfma_f32_16x16x32_bf16 v[32:35], v[124:127], a[212:215], v[32:35]\n\t" \
  "ds_read_b64_tr_b16 v[148:149], v163 offset:8192\n\t" \
  "v_mfma_f32_16x16x32_bf16 v[32:35], v[128:131], a[216:219], v[32:35]\n\t" \
  "v_exp_f32 v17, v17\n\t" \
  "v_mfma_f32_16x16x32_bf16 v[32:35], v[132:135], a[220:223], v[32:35]\n\t" \
  "ds_read_b64_tr_b16 v[150:151], v163 offset:12288\n\t" \
  "v_cvt_pk_bf16_f32 v76, v16, v17\n\t" \
  "v_mfma_f32_16x16x32_bf16 v[36:39], v[120:123], a[224:227], 0\n\t" \
  "s_waitcnt lgkmcnt(8)\n\t" \
  "v_exp_f32 v18, v18\n\t" \
  "v_mfma_f32_16x16x32_bf16 v[36:39], v[124:127], a[228:231], v[36:39]\n\t" \
  "ds_read_b64_tr_b16 v[236:237], v167 offset:8192\n\t" \
  "v_mfma_f32_16x16x32_bf16 v[36:39], v[128:131], a[232:235], v[36:39]\n\t" \
  "s_waitcnt lgkmcnt(5)\n\t" \
  "v_exp_f32 v19, v19\n\t" \
  "v_mfma_f32_16x16x32_bf16 v[36:39], v[132:135], a[236:239], v[36:39]\n\t" \
  "v_cvt_pk_bf16_f32 v77, v18, v19\n\t" \
  "v_mfma_f32_16x16x32_bf16 v[184:187], v[232:235], v[224:227], v[184:187]\n\t" \
  "ds_read_b64_tr_b16 v[238:239], v167 offset:12288\n\t" \
  "v_mfma_f32_16x16x32_bf16 a[120:123], v[136:139], v[80:83], a[120:123]\n\t" \
  "s_add_u32 m0, s74, 4096\n\t" \
  "v_exp_f32 v20, v20\n\t" \
  "v_mfma_f32_16x16x32_bf16 a[120:123], v[140:143], v[100:103], a[120:123]\n\t" \
  "buffer_load_dwordx4 v240, %[srdg], s84 offen offset:0 lds\n\t" \
  "v_mfma_f32_16x16x32_bf16 a[124:127], v[136:139], v[84:87], a[124:127]\n\t" \
  "v_exp_f32 v21, v21\n\t" \
  "v_mfma_f32_16x16x32_bf16 a[124:127], v[140:143], v[104:107], a[124:127]\n\t" \
  "buffer_load_dwordx4 v241, %[srdg], s85 offen offset:1024 lds\n\t" \
  "v_mfma_f32_16x16x32_bf16 a[128:131], v[136:139], v[88:91], a[128:131]\n\t" \
  "s_waitcnt lgkmcnt(2)\n\t" \
  "v_exp_f32 v22, v22\n\t" \
  "v_mfma_f32_16x16x32_bf16 a[128:131], v[140:143], v[108:111], a[128:131]\n\t" \
  "buffer_load_dwordx4 v242, %[srdg], s86 offen offset:2048 lds\n\t" \
  "v_mfma_f32_16x16x32_bf16 a[132:135], v[136:139], v[92:95], a[132:135]\n\t" \
  "s_waitcnt vmcnt(7)\n\t" \
  "s_barrier\n\t" \
  "v_exp_f32 v23, v23\n\t" \
  "v_mfma_f32_16x16x32_bf16 a[132:135], v[140:143], v[112:115], a[132:135]\n\t" \
  "ds_read_b128 v[120:123], v152 offset:32768\n\t" \
  "v_cvt_pk_bf16_f32 v62, v20, v21\n\t" \
  "v_mfma_f32_16x16x32_bf16 a[136:139], v[136:139], v[96:99], a[136:139]\n\t" \
  "v_exp_f32 v24, v24\n\t" \
  "v_mfma_f32_16x16x32_bf16 a[136:139], v[140:143], v[116:119], a[136:139]\n\t" \
  "ds_read_b128 v[124:127], v153 offset:32768\n\t" \
  "v_cvt_pk_bf16_f32 v63, v22, v23\n\t" \
  "v_mfma_f32_16x16x32_bf16 a[140:143], v[144:147], v[80:83], a[140:143]\n\t" \
  "v_exp_f32 v25, v25\n\t" \
  "v_mfma_f32_16x16x32_bf16 a[140:143], v[148:151], v[100:103], a[140:143]\n\t" \
  "ds_read_b128 v[128:131], v154 offset:32768\n\t" \
  "v_cvt_pk_bf16_f32 v66, v24, v25\n\t" \
  "v_mfma_f32_16x16x32_bf16 a[144:147], v[144:147], v[84:87], a[144:147]\n\t" \
  "v_exp_f32 v26, v26\n\t" \
  "v_mfma_f32_16x16x32_bf16 a[144:147], v[148:151], v[104:107], a[144:147]\n\t" \
  "ds_read_b128 v[132:135], v155 offset:32768\n\t" \
  "v_mfma_f32_16x16x32_bf16 a[148:151], v[144:147], v[88:91], a[148:151]\n\t" \
  "v_exp_f32 v27, v27\n\t" \
  "v_mfma_f32_16x16x32_bf16 a[148:151], v[148:151], v[108:111], a[148:151]\n\t" \
  "v_cvt_pk_bf16_f32 v67, v26, v27\n\t" \
  "v_mfma_f32_16x16x32_bf16 a[152:155], v[144:147], v[92:95], a[152:155]\n\t" \
  "s_waitcnt lgkmcnt(4)\n\t" \
  "v_exp_f32 v28, v28\n\t" \
  "v_mfma_f32_16x16x32_bf16 a[152:155], v[148:151], v[112:115], a[152:155]\n\t" \
  "ds_read_b64_tr_b16 v[232:233], v168 offset:8192\n\t" \
  "v_mfma_f32_16x16x32_bf16 a[156:159], v[144:147], v[96:99], a[156:159]\n\t" \
  "s_waitcnt lgkmcnt(1)\n\t" \
  "v_exp_f32 v29, v29\n\t" \
  "v_mfma_f32_16x16x32_bf16 a[156:159], v[148:151], v[116:119], a[156:159]\n\t" \
  "buffer_load_dwordx4 v243, %[srdg], s87 offen offset:3072 lds\n\t" \
  "v_mfma_f32_16x16x32_bf16 v[188:191], v[236:239], v[224:227], v[188:191]\n\t" \
  "ds_read_b64_tr_b16 v[234:235], v168 offset:12288\n\t" \
  "v_cvt_pk_bf16_f32 v70, v28, v29\n\t" \
  "v_mfma_f32_16x16x32_bf16 v[0:3], v[120:123], a[160:163], 0\n\t" \
  "v_exp_f32 v30, v30\n\t" \
  "v_mfma_f32_16x16x32_bf16 v[0:3], v[124:127], a[164:167], v[0:3]\n\t" \
  "ds_read_b64_tr_b16 v[136:137], v156 offset:16384\n\t" \
  "v_mov_b32_dpp v244, v216 row_newbcast:3 row_mask:0xf bank_mask:0xf\n\t" \
  "v_mfma_f32_16x16x32_bf16 v[0:3], v[128:131], a[168:171], v[0:3]\n\t" \
  "v_exp_f32 v31, v31\n\t" \
  "v_mfma_f32_16x16x32_bf16 v[0:3], v[132:135], a[172:175], v[0:3]\n\t" \
  "ds_read_b64_tr_b16 v[138:139], v156 offset:20480\n\t" \
  "v_cvt_pk_bf16_f32 v71, v30, v31\n\t" \
  "v_mfma_f32_16x16x32_bf16 v[4:7], v[120:123], a[176:179], 0\n\t" \
  "v_exp_f32 v32, v32\n\t" \
  "v_mfma_f32_16x16x32_bf16 v[4:7], v[124:127], a[180:183], v[4:7]\n\t" \
  "ds_read_b64_tr_b16 v[140:141], v156 offset:24576\n\t" \
  "v_alignbit_b32 v244, v244, v244, v248\n\t" \
  "v_mfma_f32_16x16x32_bf16 v[4:7], v[128:131], a[184:187], v[4:7]\n\t" \
  "v_exp_f32 v33, v33\n\t" \
  "v_mfma_f32_16x16x32_bf16 v[4:7], v[132:135], a[188:191], v[4:7]\n\t" \
  "ds_read_b64_tr_b16 v[142:143], v156 offset:28672\n\t" \
  "v_cvt_pk_bf16_f32 v74, v32, v33\n\t" \
  "v_mfma_f32_16x16x32_bf16 v[8:11], v[120:123], a[192:195], 0\n\t" \
  "v_exp_f32 v34, v34\n\t" \
  "v_mfma_f32_16x16x32_bf16 v[8:11], v[124:127], a[196:199], v[8:11]\n\t" \
  "ds_read_b64_tr_b16 v[144:145], v157 offset:16384\n\t" \
  "v_and_b32 v220, v244, v249\n\t" \
  "v_mfma_f32_16x16x32_bf16 v[8:11], v[128:131], a[200:203], v[8:11]\n\t" \
  "v_exp_f32 v35, v35\n\t" \
  "v_mfma_f32_16x16x32_bf16 v[8:11], v[132:135], a[204:207], v[8:11]\n\t" \
  "ds_read_b64_tr_b16 v[146:147], v157 offset:20480\n\t" \
  "v_cvt_pk_bf16_f32 v75, v34, v35\n\t" \
  "v_mfma_f32_16x16x32_bf16 v[12:15], v[120:123], a[208:211], 0\n\t" \
  "v_exp_f32 v36, v36\n\t" \
  "v_mfma_f32_16x16x32_bf16 v[12:15], v[124:127], a[212:215], v[12:15]\n\t" \
  "ds_read_b64_tr_b16 v[148:149], v157 offset:24576\n\t" \
  "v_mov_b32_dpp v245, v217 row_newbcast:3 row_mask:0xf bank_mask:0xf\n\t" \
  "v_mfma_f32_16x16x32_bf16 v[12:15], v[128:131], a[216:219], v[12:15]\n\t" \
  "v_exp_f32 v37, v37\n\t" \
  "v_mfma_f32_16x16x32_bf16 v[12:15], v[132:135], a[220:223], v[12:15]\n\t" \
  "ds_read_b64_tr_b16 v[150:151], v157 offset:28672\n\t" \
  "v_cvt_pk_bf16_f32 v78, v36, v37\n\t" \
  "v_mfma_f32_16x16x32_bf16 v[16:19], v[120:123], a[224:227], 0\n\t" \
  "s_waitcnt lgkmcnt(8)\n\t" \
  "v_exp_f32 v38, v38\n\t" \
  "v_mfma_f32_16x16x32_bf16 v[16:19], v[124:127], a[228:231], v[16:19]\n\t" \
  "ds_read_b64_tr_b16 v[236:237], v169 offset:8192\n\t" \
  "v_alignbit_b32 v245, v245, v245, v248\n\t" \
  "v_mfma_f32_16x16x32_bf16 v[16:19], v[128:131], a[232:235], v[16:19]\n\t" \
  "s_waitcnt lgkmcnt(5)\n\t" \
  "v_exp_f32 v39, v39\n\t" \
  "v_mfma_f32_16x16x32_bf16 v[16:19], v[132:135], a[236:239], v[16:19]\n\t" \
  "v_cvt_pk_bf16_f32 v79, v38, v39\n\t" \
  "v_and_b32 v221, v245, v249\n\t" \
  "v_mfma_f32_16x16x32_bf16 v[192:195], v[232:235], v[224:227], v[192:195]\n\t" \
  "ds_read_b64_tr_b16 v[238:239], v169 offset:12288\n\t" \
  "v_mfma_f32_16x16x32_bf16 a[0:3], v[136:139], v[40:43], a[0:3]\n\t" \
  "v_exp_f32 v0, v0\n\t" \
  "v_mfma_f32_16x16x32_bf16 a[0:3], v[140:143], v[60:63], a[0:3]\n\t" \
  "ds_read_b128 v[120:123], v152 offset:36864\n\t" \
  "v_mfma_f32_16x16x32_bf16 a[4:7], v[136:139], v[44:47], a[4:7]\n\t" \
  "v_exp_f32 v1, v1\n\t" \
  "v_mfma_f32_16x16x32_bf16 a[4:7], v[140:143], v[64:67], a[4:7]\n\t" \
  "ds_read_b128 v[124:127], v153 offset:36864\n\t" \
  "v_cvt_pk_bf16_f32 v80, v0, v1\n\t" \
  "v_mfma_f32_16x16x32_bf16 a[8:11], v[136:139], v[48:51], a[8:11]\n\t" \
  "s_waitcnt lgkmcnt(4)\n\t" \
  "v_exp_f32 v2, v2\n\t" \
  "v_mfma_f32_16x16x32_bf16 a[8:11], v[140:143], v[68:71], a[8:11]\n\t" \
  "ds_read_b128 v[128:131], v154 offset:36864\n\t" \
  "v_mfma_f32_16x16x32_bf16 a[12:15], v[136:139], v[52:55], a[12:15]\n\t" \
  "s_add_u32 %[soff], %[soff], 12288\n\t" \
  "v_exp_f32 v3, v3\n\t" \
  "v_mfma_f32_16x16x32_bf16 a[12:15], v[140:143], v[72:75], a[12:15]\n\t" \
  "ds_read_b128 v[132:135], v155 offset:36864\n\t" \
  "v_cvt_pk_bf16_f32 v81, v2, v3\n\t" \
  "v_mfma_f32_16x16x32_bf16 a[16:19], v[136:139], v[56:59], a[16:19]\n\t" \
  "s_add_u32 m0, s72, 0\n\t" \
  "v_exp_f32 v4, v4\n\t" \
  "v_mfma_f32_16x16x32_bf16 a[16:19], v[140:143], v[76:79], a[16:19]\n\t" \
  "buffer_load_dwordx4 v172, %[srd], %[soff] offen offset:0 lds\n\t" \
  "v_mfma_f32_16x16x32_bf16 a[20:23], v[144:147], v[40:43], a[20:23]\n\t" \
  "v_exp_f32 v5, v5\n\t" \
  "v_mfma_f32_16x16x32_bf16 a[20:23], v[148:151], v[60:63], a[20:23]\n\t" \
  "buffer_load_dwordx4 v172, %[srd], %[soff] offen offset:1024 lds\n\t" \
  "v_mfma_f32_16x16x32_bf16 a[24:27], v[144:147], v[44:47], a[24:27]\n\t" \
  "v_exp_f32 v6, v6\n\t" \
  "v_mfma_f32_16x16x32_bf16 a[24:27], v[148:151], v[64:67], a[24:27]\n\t" \
  "buffer_load_dwordx4 v172, %[srd], %[soff] offen offset:2048 lds\n\t" \
  "v_mfma_f32_16x16x32_bf16 a[28:31], v[144:147], v[48:51], a[28:31]\n\t" \
  "v_exp_f32 v7, v7\n\t" \
  "v_mfma_f32_16x16x32_bf16 a[28:31], v[148:151], v[68:71], a[28:31]\n\t" \
  "v_cvt_pk_bf16_f32 v84, v4, v5\n\t" \
  "v_cvt_pk_bf16_f32 v85, v6, v7\n\t" \
  "v_mfma_f32_16x16x32_bf16 a[32:35], v[144:147], v[52:55], a[32:35]\n\t" \
  "s_waitcnt lgkmcnt(4)\n\t" \
  "v_exp_f32 v8, v8\n\t" \
  "v_mfma_f32_16x16x32_bf16 a[32:35], v[148:151], v[72:75], a[32:35]\n\t" \
  "ds_read_b64_tr_b16 v[232:233], v170 offset:8192\n\t" \
  "v_mfma_f32_16x16x32_bf16 a[36:39], v[144:147], v[56:59], a[36:39]\n\t" \
  "s_waitcnt lgkmcnt(1)\n\t" \
  "v_exp_f32 v9, v9\n\t" \
  "v_mfma_f32_16x16x32_bf16 a[36:39], v[148:151], v[76:79], a[36:39]\n\t" \
  "buffer_load_dwordx4 v172, %[srd], %[soff] offen offset:3072 lds\n\t" \
  "v_mfma_f32_16x16x32_bf16 v[196:199], v[236:239], v[224:227], v[196:199]\n\t" \
  "ds_read_b64_tr_b16 v[234:235], v170 offset:12288\n\t" \
  "v_cvt_pk_bf16_f32 v88, v8, v9\n\t" \
  "v_mfma_f32_16x16x32_bf16 v[20:23], v[120:123], a[160:163], 0\n\t" \
  "v_exp_f32 v10, v10\n\t" \
  "v_mfma_f32_16x16x32_bf16 v[20:23], v[124:127], a[164:167], v[20:23]\n\t" \
  "ds_read_b64_tr_b16 v[136:137], v158 offset:16384\n\t" \
  "v_mov_b32_dpp v244, v218 row_newbcast:3 row_mask:0xf bank_mask:0xf\n\t" \
  "v_mfma_f32_16x16x32_bf16 v[20:23], v[128:131], a[168:171], v[20:23]\n\t" \
  "v_exp_f32 v11, v11\n\t" \
  "v_mfma_f32_16x16x32_bf16 v[20:23], v[132:135], a[172:175], v[20:23]\n\t" \
  "ds_read_b64_tr_b16 v[138:139], v158 offset:20480\n\t" \
  "v_cvt_pk_bf16_f32 v89, v10, v11\n\t" \
  "v_mfma_f32_16x16x32_bf16 v[24:27], v[120:123], a[176:179], 0\n\t" \
  "v_exp_f32 v12, v12\n\t" \
  "v_mfma_f32_16x16x32_bf16 v[24:27], v[124:127], a[180:183], v[24:27]\n\t" \
  "ds_read_b64_tr_b16 v[140:141], v158 offset:24576\n\t" \
  "v_alignbit_b32 v244, v244, v244, v248\n\t" \
  "v_mfma_f32_16x16x32_bf16 v[24:27], v[128:131], a[184:187], v[24:27]\n\t" \
  "v_exp_f32 v13, v13\n\t" \
  "v_mfma_f32_16x16x32_bf16 v[24:27], v[132:135], a[188:191], v[24:27]\n\t" \
  "ds_read_b64_tr_b16 v[142:143], v158 offset:28672\n\t" \
  "v_cvt_pk_bf16_f32 v92, v12, v13\n\t" \
  "v_mfma_f32_16x16x32_bf16 v[28:31], v[120:123], a[192:195], 0\n\t" \
  "v_exp_f32 v14, v14\n\t" \
  "v_mfma_f32_16x16x32_bf16 v[28:31], v[124:127], a[196:199], v[28:31]\n\t" \
  "ds_read_b64_tr_b16 v[144:145], v159 offset:16384\n\t" \
  "v_and_b32 v222, v244, v249\n\t" \
  "v_mfma_f32_16x16x32_bf16 v[28:31], v[128:131], a[200:203], v[28:31]\n\t" \
  "v_exp_f32 v15, v15\n\t" \
  "v_mfma_f32_16x16x32_bf16 v[28:31], v[132:135], a[204:207], v[28:31]\n\t" \
  "ds_read_b64_tr_b16 v[146:147], v159 offset:20480\n\t" \
  "v_cvt_pk_bf16_f32 v93, v14, v15\n\t" \
  "v_mfma_f32_16x16x32_bf16 v[32:35], v[120:123], a[208:211], 0\n\t" \
  "v_exp_f32 v16, v16\n\t" \
  "v_mfma_f32_16x16x32_bf16 v[32:35], v[124:127], a[212:215], v[32:35]\n\t" \
  "ds_read_b64_tr_b16 v[148:149], v159 offset:24576\n\t" \
  "v_mov_b32_dpp v245, v219 row_newbcast:3 row_mask:0xf bank_mask:0xf\n\t" \
  "v_mfma_f32_16x16x32_bf16 v[32:35], v[128:131], a[216:219], v[32:35]\n\t" \
  "v_exp_f32 v17, v17\n\t" \
  "v_mfma_f32_16x16x32_bf16 v[32:35], v[132:135], a[220:223], v[32:35]\n\t" \
  "ds_read_b64_tr_b16 v[150:151], v159 offset:28672\n\t" \
  "v_cvt_pk_bf16_f32 v96, v16, v17\n\t" \
  "v_mfma_f32_16x16x32_bf16 v[36:39], v[120:123], a[224:227], 0\n\t" \
  "s_waitcnt lgkmcnt(8)\n\t" \
  "v_exp_f32 v18, v18\n\t" \
  "v_mfma_f32_16x16x32_bf16 v[36:39], v[124:127], a[228:231], v[36:39]\n\t" \
  "ds_read_b64_tr_b16 v[236:237], v171 offset:8192\n\t" \
  "v_alignbit_b32 v245, v245, v245, v248\n\t" \
  "v_mfma_f32_16x16x32_bf16 v[36:39], v[128:131], a[232:235], v[36:39]\n\t" \
  "s_waitcnt lgkmcnt(5)\n\t" \
  "v_exp_f32 v19, v19\n\t" \
  "v_mfma_f32_16x16x32_bf16 v[36:39], v[132:135], a[236:239], v[36:39]\n\t" \
  "v_cvt_pk_bf16_f32 v97, v18, v19\n\t" \
  "v_and_b32 v223, v245, v249\n\t" \
  "v_mfma_f32_16x16x32_bf16 v[200:203], v[232:235], v[224:227], v[200:203]\n\t" \
  "ds_read_b64_tr_b16 v[238:239], v171 offset:12288\n\t" \
  "v_mfma_f32_16x16x32_bf16 a[40:43], v[136:139], v[40:43], a[40:43]\n\t" \
  "v_exp_f32 v20, v20\n\t" \
  "v_mfma_f32_16x16x32_bf16 a[40:43], v[140:143], v[60:63], a[40:43]\n\t" \
  "ds_read_b128 v[120:123], v152 offset:40960\n\t" \
  "v_xor_b32_dpp v240, v208, v247 row_newbcast:3 row_mask:0xf bank_mask:0xf\n\t" \
  "v_mfma_f32_16x16x32_bf16 a[44:47], v[136:139], v[44:47], a[44:47]\n\t" \
  "v_exp_f32 v21, v21\n\t" \
  "v_mfma_f32_16x16x32_bf16 a[44:47], v[140:143], v[64:67], a[44:47]\n\t" \
  "ds_read_b128 v[124:127], v153 offset:40960\n\t" \
  "v_cvt_pk_bf16_f32 v82, v20, v21\n\t" \
  "v_mfma_f32_16x16x32_bf16 a[48:51], v[136:139], v[48:51], a[48:51]\n\t" \
  "s_waitcnt lgkmcnt(4)\n\t" \
  "v_exp_f32 v22, v22\n\t" \
  "v_mfma_f32_16x16x32_bf16 a[48:51], v[140:143], v[68:71], a[48:51]\n\t" \
  "ds_read_b128 v[128:131], v154 offset:40960\n\t" \
  "v_xor_b32_dpp v241, v209, v247 row_newbcast:3 row_mask:0xf bank_mask:0xf\n\t" \
  "v_mfma_f32_16x16x32_bf16 a[52:55], v[136:139], v[52:55], a[52:55]\n\t" \
  "s_add_u32 %[soff], %[soff], 4096\n\t" \
  "v_exp_f32 v23, v23\n\t" \
  "v_mfma_f32_16x16x32_bf16 a[52:55], v[140:143], v[72:75], a[52:55]\n\t" \
  "ds_read_b128 v[132:135], v155 offset:40960\n\t" \
  "v_cvt_pk_bf16_f32 v83, v22, v23\n\t" \
  "v_mfma_f32_16x16x32_bf16 a[56:59], v[136:139], v[56:59], a[56:59]\n\t" \
  "s_add_u32 m0, s72, 4096\n\t" \
  "v_exp_f32 v24, v24\n\t" \
  "v_mfma_f32_16x16x32_bf16 a[56:59], v[140:143], v[76:79], a[56:59]\n\t" \
  "buffer_load_dwordx4 v172, %[srd], %[soff] offen offset:0 lds\n\t" \
  "v_mfma_f32_16x16x32_bf16 a[60:63], v[144:147], v[40:43], a[60:63]\n\t" \
  "v_exp_f32 v25, v25\n\t" \
  "v_mfma_f32_16x16x32_bf16 a[60:63], v[148:151], v[60:63], a[60:63]\n\t" \
  "buffer_load_dwordx4 v172, %[srd], %[soff] offen offset:1024 lds\n\t" \
  "v_mfma_f32_16x16x32_bf16 a[64:67], v[144:147], v[44:47], a[64:67]\n\t" \
  "v_exp_f32 v26, v26\n\t" \
  "v_mfma_f32_16x16x32_bf16 a[64:67], v[148:151], v[64:67], a[64:67]\n\t" \
  "buffer_load_dwordx4 v172, %[srd], %[soff] offen offset:2048 lds\n\t" \
  "v_mfma_f32_16x16x32_bf16 a[68:71], v[144:147], v[48:51], a[68:71]\n\t" \
  "s_waitcnt vmcnt(7)\n\t" \
  "v_exp_f32 v27, v27\n\t" \
  "v_mfma_f32_16x16x32_bf16 a[68:71], v[148:151], v[68:71], a[68:71]\n\t" \
  "v_cvt_pk_bf16_f32 v86, v24, v25\n\t" \
  "v_cvt_pk_bf16_f32 v87, v26, v27\n\t" \
  "v_mfma_f32_16x16x32_bf16 a[72:75], v[144:147], v[52:55], a[72:75]\n\t" \
  "s_waitcnt lgkmcnt(4)\n\t" \
  "v_exp_f32 v28, v28\n\t" \
  "v_mfma_f32_16x16x32_bf16 a[72:75], v[148:151], v[72:75], a[72:75]\n\t" \
  "ds_read_b64_tr_b16 v[232:233], v164 offset:0\n\t" \
  "v_xor_b32_dpp v242, v210, v247 row_newbcast:3 row_mask:0xf bank_mask:0xf\n\t" \
  "v_mfma_f32_16x16x32_bf16 a[76:79], v[144:147], v[56:59], a[76:79]\n\t" \
  "s_waitcnt lgkmcnt(1)\n\t" \
  "v_exp_f32 v29, v29\n\t" \
  "v_mfma_f32_16x16x32_bf16 a[76:79], v[148:151], v[76:79], a[76:79]\n\t" \
  "buffer_load_dwordx4 v172, %[srd], %[soff] offen offset:3072 lds\n\t" \
  "v_mfma_f32_16x16x32_bf16 v[204:207], v[236:239], v[224:227], v[204:207]\n\t" \
  "ds_read_b64_tr_b16 v[234:235], v164 offset:4096\n\t" \
  "v_cvt_pk_bf16_f32 v90, v28, v29\n\t" \
  "v_mfma_f32_16x16x32_bf16 v[0:3], v[120:123], a[160:163], 0\n\t" \
  "v_exp_f32 v30, v30\n\t" \
  "v_mfma_f32_16x16x32_bf16 v[0:3], v[124:127], a[164:167], v[0:3]\n\t" \
  "ds_read_b64_tr_b16 v[136:137], v160 offset:16384\n\t" \
  "v_xor_b32_dpp v243, v211, v247 row_newbcast:3 row_mask:0xf bank_mask:0xf\n\t" \
  "v_mfma_f32_16x16x32_bf16 v[0:3], v[128:131], a[168:171], v[0:3]\n\t" \
  "v_exp_f32 v31, v31\n\t" \
  "v_mfma_f32_16x16x32_bf16 v[0:3], v[132:135], a[172:175], v[0:3]\n\t" \
  "ds_read_b64_tr_b16 v[138:139], v160 offset:20480\n\t" \
  "v_cvt_pk_bf16_f32 v91, v30, v31\n\t" \
  "v_mfma_f32_16x16x32_bf16 v[4:7], v[120:123], a[176:179], 0\n\t" \
  "v_exp_f32 v32, v32\n\t" \
  "v_mfma_f32_16x16x32_bf16 v[4:7], v[124:127], a[180:183], v[4:7]\n\t" \
  "ds_read_b64_tr_b16 v[140:141], v160 offset:24576\n\t" \
  "v_mfma_f32_16x16x32_bf16 v[4:7], v[128:131], a[184:187], v[4:7]\n\t" \
  "v_exp_f32 v33, v33\n\t" \
  "v_mfma_f32_16x16x32_bf16 v[4:7], v[132:135], a[188:191], v[4:7]\n\t" \
  "ds_read_b64_tr_b16 v[142:143], v160 offset:28672\n\t" \
  "v_cvt_pk_bf16_f32 v94, v32, v33\n\t" \
  "v_mfma_f32_16x16x32_bf16 v[8:11], v[120:123], a[192:195], 0\n\t" \
  "v_exp_f32 v34, v34\n\t" \
  "v_mfma_f32_16x16x32_bf16 v[8:11], v[124:127], a[196:199], v[8:11]\n\t" \
  "ds_read_b64_tr_b16 v[144:145], v161 offset:16384\n\t" \
  "v_mfma_f32_16x16x32_bf16 v[8:11], v[128:131], a[200:203], v[8:11]\n\t" \
  "v_exp_f32 v35, v35\n\t" \
  "v_mfma_f32_16x16x32_bf16 v[8:11], v[132:135], a[204:207], v[8:11]\n\t" \
  "ds_read_b64_tr_b16 v[146:147], v161 offset:20480\n\t" \
  "v_cvt_pk_bf16_f32 v95, v34, v35\n\t" \
  "v_mfma_f32_16x16x32_bf16 v[12:15], v[120:123], a[208:211], 0\n\t" \
  "v_exp_f32 v36, v36\n\t" \
  "v_mfma_f32_16x16x32_bf16 v[12:15], v[124:127], a[212:215], v[12:15]\n\t" \
  "ds_read_b64_tr_b16 v[148:149], v161 offset:24576\n\t" \
  "v_mfma_f32_16x16x32_bf16 v[12:15], v[128:131], a[216:219], v[12:15]\n\t" \
  "v_exp_f32 v37, v37\n\t" \
  "v_mfma_f32_16x16x32_bf16 v[12:15], v[132:135], a[220:223], v[12:15]\n\t" \
  "ds_read_b64_tr_b16 v[150:151], v161 offset:28672\n\t" \
  "v_cvt_pk_bf16_f32 v98, v36, v37\n\t" \
  "v_mfma_f32_16x16x32_bf16 v[16:19], v[120:123], a[224:227], 0\n\t" \
  "s_waitcnt lgkmcnt(8)\n\t" \
  "v_exp_f32 v38, v38\n\t" \
  "v_mfma_f32_16x16x32_bf16 v[16:19], v[124:127], a[228:231], v[16:19]\n\t" \
  "ds_read_b64_tr_b16 v[236:237], v165 offset:0\n\t" \
  "v_mfma_f32_16x16x32_bf16 v[16:19], v[128:131], a[232:235], v[16:19]\n\t" \
  "s_waitcnt lgkmcnt(5)\n\t" \
  "v_exp_f32 v39, v39\n\t" \
  "v_mfma_f32_16x16x32_bf16 v[16:19], v[132:135], a[236:239], v[16:19]\n\t" \
  "v_cvt_pk_bf16_f32 v99, v38, v39\n\t" \
  "v_mfma_f32_16x16x32_bf16 v[176:179], v[232:235], v[228:231], v[176:179]\n\t" \
  "ds_read_b64_tr_b16 v[238:239], v165 offset:4096\n\t" \
  "v_mfma_f32_16x16x32_bf16 a[80:83], v[136:139], v[40:43], a[80:83]\n\t" \
  "v_exp_f32 v0, v0\n\t" \
  "v_mfma_f32_16x16x32_bf16 a[80:83], v[140:143], v[60:63], a[80:83]\n\t" \
  "ds_read_b128 v[120:123], v152 offset:45056\n\t" \
  "v_mfma_f32_16x16x32_bf16 a[84:87], v[136:139], v[44:47], a[84:87]\n\t" \
  "v_exp_f32 v1, v1\n\t" \
  "v_mfma_f32_16x16x32_bf16 a[84:87], v[140:143], v[64:67], a[84:87]\n\t" \
  "ds_read_b128 v[124:127], v153 offset:45056\n\t" \
  "v_cvt_pk_bf16_f32 v100, v0, v1\n\t" \
  "v_mfma_f32_16x16x32_bf16 a[88:91], v[136:139], v[48:51], a[88:91]\n\t" \
  "s_waitcnt lgkmcnt(4)\n\t" \
  "v_exp_f32 v2, v2\n\t" \
  "v_mfma_f32_16x16x32_bf16 a[88:91], v[140:143], v[68:71], a[88:91]\n\t" \
  "ds_read_b128 v[128:131], v154 offset:45056\n\t" \
  "v_mfma_f32_16x16x32_bf16 a[92:95], v[136:139], v[52:55], a[92:95]\n\t" \
  "v_exp_f32 v3, v3\n\t" \
  "v_mfma_f32_16x16x32_bf16 a[92:95], v[140:143], v[72:75], a[92:95]\n\t" \
  "ds_read_b128 v[132:135], v155 offset:45056\n\t" \
  "v_cvt_pk_bf16_f32 v101, v2, v3\n\t" \
  "v_mfma_f32_16x16x32_bf16 a[96:99], v[136:139], v[56:59], a[96:99]\n\t" \
  "s_add_u32 m0, s74, 8192\n\t" \
  "v_exp_f32 v4, v4\n\t" \
  "v_mfma_f32_16x16x32_bf16 a[96:99], v[140:143], v[76:79], a[96:99]\n\t" \
  "buffer_load_dwordx4 v240, %[srdg], s84 offen offset:0 lds\n\t" \
  "v_mfma_f32_16x16x32_bf16 a[100:103], v[144:147], v[40:43], a[100:103]\n\t" \
  "v_exp_f32 v5, v5\n\t" \
  "v_mfma_f32_16x16x32_bf16 a[100:103], v[148:151], v[60:63], a[100:103]\n\t" \
  "buffer_load_dwordx4 v241, %[srdg], s85 offen offset:1024 lds\n\t" \
  "v_mfma_f32_16x16x32_bf16 a[104:107], v[144:147], v[44:47], a[104:107]\n\t" \
  "v_exp_f32 v6, v6\n\t" \
  "v_mfma_f32_16x16x32_bf16 a[104:107], v[148:151], v[64:67], a[104:107]\n\t" \
  "buffer_load_dwordx4 v242, %[srdg], s86 offen offset:2048 lds\n\t" \
  "v_mfma_f32_16x16x32_bf16 a[108:111], v[144:147], v[48:51], a[108:111]\n\t" \
  "v_exp_f32 v7, v7\n\t" \
  "v_mfma_f32_16x16x32_bf16 a[108:111], v[148:151], v[68:71], a[108:111]\n\t" \
  "v_cvt_pk_bf16_f32 v104, v4, v5\n\t" \
  "v_cvt_pk_bf16_f32 v105, v6, v7\n\t" \
  "v_mfma_f32_16x16x32_bf16 a[112:115], v[144:147], v[52:55], a[112:115]\n\t" \
  "s_waitcnt lgkmcnt(4)\n\t" \
  "v_exp_f32 v8, v8\n\t" \
  "v_mfma_f32_16x16x32_bf16 a[112:115], v[148:151], v[72:75], a[112:115]\n\t" \
  "ds_read_b64_tr_b16 v[232:233], v166 offset:0\n\t" \
  "v_xor_b32_dpp v240, v212, v247 row_newbcast:3 row_mask:0xf bank_mask:0xf\n\t" \
  "v_mfma_f32_16x16x32_bf16 a[116:119], v[144:147], v[56:59], a[116:119]\n\t" \
  "s_waitcnt lgkmcnt(1)\n\t" \
  "v_exp_f32 v9, v9\n\t" \
  "v_mfma_f32_16x16x32_bf16 a[116:119], v[148:151], v[76:79], a[116:119]\n\t" \
  "buffer_load_dwordx4 v243, %[srdg], s87 offen offset:3072 lds\n\t" \
  "v_mfma_f32_16x16x32_bf16 v[180:183], v[236:239], v[228:231], v[180:183]\n\t" \
  "ds_read_b64_tr_b16 v[234:235], v166 offset:4096\n\t" \
  "v_cvt_pk_bf16_f32 v108, v8, v9\n\t" \
  "v_mfma_f32_16x16x32_bf16 v[20:23], v[120:123], a[160:163], 0\n\t" \
  "v_exp_f32 v10, v10\n\t" \
  "v_mfma_f32_16x16x32_bf16 v[20:23], v[124:127], a[164:167], v[20:23]\n\t" \
  "ds_read_b64_tr_b16 v[136:137], v162 offset:16384\n\t" \
  "v_xor_b32_dpp v241, v213, v247 row_newbcast:3 row_mask:0xf bank_mask:0xf\n\t" \
  "v_mfma_f32_16x16x32_bf16 v[20:23], v[128:131], a[168:171], v[20:23]\n\t" \
  "v_exp_f32 v11, v11\n\t" \
  "v_mfma_f32_16x16x32_bf16 v[20:23], v[132:135], a[172:175], v[20:23]\n\t" \
  "ds_read_b64_tr_b16 v[138:139], v162 offset:20480\n\t" \
  "v_cvt_pk_bf16_f32 v109, v10, v11\n\t" \
  "v_mfma_f32_16x16x32_bf16 v[24:27], v[120:123], a[176:179], 0\n\t" \
  "v_exp_f32 v12, v12\n\t" \
  "v_mfma_f32_16x16x32_bf16 v[24:27], v[124:127], a[180:183], v[24:27]\n\t" \
  "ds_read_b64_tr_b16 v[140:141], v162 offset:24576\n\t" \
  "v_xor_b32_dpp v242, v214, v247 row_newbcast:3 row_mask:0xf bank_mask:0xf\n\t" \
  "v_mfma_f32_16x16x32_bf16 v[24:27], v[128:131], a[184:187], v[24:27]\n\t" \
  "v_exp_f32 v13, v13\n\t" \
  "v_mfma_f32_16x16x32_bf16 v[24:27], v[132:135], a[188:191], v[24:27]\n\t" \
  "ds_read_b64_tr_b16 v[142:143], v162 offset:28672\n\t" \
  "v_cvt_pk_bf16_f32 v112, v12, v13\n\t" \
  "v_mfma_f32_16x16x32_bf16 v[28:31], v[120:123], a[192:195], 0\n\t" \
  "v_exp_f32 v14, v14\n\t" \
  "v_mfma_f32_16x16x32_bf16 v[28:31], v[124:127], a[196:199], v[28:31]\n\t" \
  "ds_read_b64_tr_b16 v[144:145], v163 offset:16384\n\t" \
  "v_xor_b32_dpp v243, v215, v247 row_newbcast:3 row_mask:0xf bank_mask:0xf\n\t" \
  "v_mfma_f32_16x16x32_bf16 v[28:31], v[128:131], a[200:203], v[28:31]\n\t" \
  "v_exp_f32 v15, v15\n\t" \
  "v_mfma_f32_16x16x32_bf16 v[28:31], v[132:135], a[204:207], v[28:31]\n\t" \
  "ds_read_b64_tr_b16 v[146:147], v163 offset:20480\n\t" \
  "v_cvt_pk_bf16_f32 v113, v14, v15\n\t" \
  "v_mfma_f32_16x16x32_bf16 v[32:35], v[120:123], a[208:211], 0\n\t" \
  "v_exp_f32 v16, v16\n\t" \
  "v_mfma_f32_16x16x32_bf16 v[32:35], v[124:127], a[212:215], v[32:35]\n\t" \
  "ds_read_b64_tr_b16 v[148:149], v163 offset:24576\n\t" \
  "v_mfma_f32_16x16x32_bf16 v[32:35], v[128:131], a[216:219], v[32:35]\n\t" \
  "v_exp_f32 v17, v17\n\t" \
  "v_mfma_f32_16x16x32_bf16 v[32:35], v[132:135], a[220:223], v[32:35]\n\t" \
  "ds_read_b64_tr_b16 v[150:151], v163 offset:28672\n\t" \
  "v_cvt_pk_bf16_f32 v116, v16, v17\n\t" \
  "v_mfma_f32_16x16x32_bf16 v[36:39], v[120:123], a[224:227], 0\n\t" \
  "s_waitcnt lgkmcnt(8)\n\t" \
  "v_exp_f32 v18, v18\n\t" \
  "v_mfma_f32_16x16x32_bf16 v[36:39], v[124:127], a[228:231], v[36:39]\n\t" \
  "ds_read_b64_tr_b16 v[236:237], v167 offset:0\n\t" \
  "v_mfma_f32_16x16x32_bf16 v[36:39], v[128:131], a[232:235], v[36:39]\n\t" \
  "s_waitcnt lgkmcnt(5)\n\t" \
  "v_exp_f32 v19, v19\n\t" \
  "v_mfma_f32_16x16x32_bf16 v[36:39], v[132:135], a[236:239], v[36:39]\n\t" \
  "v_cvt_pk_bf16_f32 v117, v18, v19\n\t" \
  "v_mfma_f32_16x16x32_bf16 v[184:187], v[232:235], v[228:231], v[184:187]\n\t" \
  "ds_read_b64_tr_b16 v[238:239], v167 offset:4096\n\t" \
  "v_mfma_f32_16x16x32_bf16 a[120:123], v[136:139], v[40:43], a[120:123]\n\t" \
  "s_add_u32 m0, s74, 12288\n\t" \
  "v_exp_f32 v20, v20\n\t" \
  "v_mfma_f32_16x16x32_bf16 a[120:123], v[140:143], v[60:63], a[120:123]\n\t" \
  "buffer_load_dwordx4 v240, %[srdg], s84 offen offset:0 lds\n\t" \
  "v_mfma_f32_16x16x32_bf16 a[124:127], v[136:139], v[44:47], a[124:127]\n\t" \
  "v_exp_f32 v21, v21\n\t" \
  "v_mfma_f32_16x16x32_bf16 a[124:127], v[140:143], v[64:67], a[124:127]\n\t" \
  "buffer_load_dwordx4 v241, %[srdg], s85 offen offset:1024 lds\n\t" \
  "v_mfma_f32_16x16x32_bf16 a[128:131], v[136:139], v[48:51], a[128:131]\n\t" \
  "s_waitcnt lgkmcnt(2)\n\t" \
  "v_exp_f32 v22, v22\n\t" \
  "v_mfma_f32_16x16x32_bf16 a[128:131], v[140:143], v[68:71], a[128:131]\n\t" \
  "buffer_load_dwordx4 v242, %[srdg], s86 offen offset:2048 lds\n\t" \
  "v_mfma_f32_16x16x32_bf16 a[132:135], v[136:139], v[52:55], a[132:135]\n\t" \
  "s_waitcnt vmcnt(7)\n\t" \
  "s_barrier\n\t" \
  "v_exp_f32 v23, v23\n\t" \
  "v_mfma_f32_16x16x32_bf16 a[132:135], v[140:143], v[72:75], a[132:135]\n\t" \
  "ds_read_b128 v[120:123], v152 offset:0\n\t" \
  "v_cvt_pk_bf16_f32 v102, v20, v21\n\t" \
  "v_mfma_f32_16x16x32_bf16 a[136:139], v[136:139], v[56:59], a[136:139]\n\t" \
  "v_exp_f32 v24, v24\n\t" \
  "v_mfma_f32_16x16x32_bf16 a[136:139], v[140:143], v[76:79], a[136:139]\n\t" \
  "ds_read_b128 v[124:127], v153 offset:0\n\t" \
  "v_cvt_pk_bf16_f32 v103, v22, v23\n\t" \
  "v_mfma_f32_16x16x32_bf16 a[140:143], v[144:147], v[40:43], a[140:143]\n\t" \
  "v_exp_f32 v25, v25\n\t" \
  "v_mfma_f32_16x16x32_bf16 a[140:143], v[148:151], v[60:63], a[140:143]\n\t" \
  "ds_read_b128 v[128:131], v154 offset:0\n\t" \
  "v_cvt_pk_bf16_f32 v106, v24, v25\n\t" \
  "v_mfma_f32_16x16x32_bf16 a[144:147], v[144:147], v[44:47], a[144:147]\n\t" \
  "v_exp_f32 v26, v26\n\t" \
  "v_mfma_f32_16x16x32_bf16 a[144:147], v[148:151], v[64:67], a[144:147]\n\t" \
  "ds_read_b128 v[132:135], v155 offset:0\n\t" \
  "v_mfma_f32_16x16x32_bf16 a[148:151], v[144:147], v[48:51], a[148:151]\n\t" \
  "v_exp_f32 v27, v27\n\t" \
  "v_mfma_f32_16x16x32_bf16 a[148:151], v[148:151], v[68:71], a[148:151]\n\t" \
  "v_cvt_pk_bf16_f32 v107, v26, v27\n\t" \
  "v_mfma_f32_16x16x32_bf16 a[152:155], v[144:147], v[52:55], a[152:155]\n\t" \
  "s_waitcnt lgkmcnt(4)\n\t" \
  "v_exp_f32 v28, v28\n\t" \
  "v_mfma_f32_16x16x32_bf16 a[152:155], v[148:151], v[72:75], a[152:155]\n\t" \
  "ds_read_b64_tr_b16 v[232:233], v168 offset:0\n\t" \
  "v_mfma_f32_16x16x32_bf16 a[156:159], v[144:147], v[56:59], a[156:159]\n\t" \
  "s_waitcnt lgkmcnt(1)\n\t" \
  "v_exp_f32 v29, v29\n\t" \
  "v_mfma_f32_16x16x32_bf16 a[156:159], v[148:151], v[76:79], a[156:159]\n\t" \
  "buffer_load_dwordx4 v243, %[srdg], s87 offen offset:3072 lds\n\t" \
  "v_mfma_f32_16x16x32_bf16 v[188:191], v[236:239], v[228:231], v[188:191]\n\t" \
  "ds_read_b64_tr_b16 v[234:235], v168 offset:4096\n\t" \
  "v_cvt_pk_bf16_f32 v110, v28, v29\n\t" \
  "v_mfma_f32_16x16x32_bf16 v[0:3], v[120:123], a[160:163], 0\n\t" \
  "v_exp_f32 v30, v30\n\t" \
  "v_mfma_f32_16x16x32_bf16 v[0:3], v[124:127], a[164:167], v[0:3]\n\t" \
  "ds_read_b64_tr_b16 v[136:137], v156 offset:32768\n\t" \
  "v_mov_b32_dpp v244, v216 row_newbcast:4 row_mask:0xf bank_mask:0xf\n\t" \
  "v_mfma_f32_16x16x32_bf16 v[0:3], v[128:131], a[168:171], v[0:3]\n\t" \
  "v_exp_f32 v31, v31\n\t" \
  "v_mfma_f32_16x16x32_bf16 v[0:3], v[132:135], a[172:175], v[0:3]\n\t" \
  "ds_read_b64_tr_b16 v[138:139], v156 offset:36864\n\t" \
  "v_cvt_pk_bf16_f32 v111, v30, v31\n\t" \
  "v_mfma_f32_16x16x32_bf16 v[4:7], v[120:123], a[176:179], 0\n\t" \
  "v_exp_f32 v32, v32\n\t" \
  "v_mfma_f32_16x16x32_bf16 v[4:7], v[124:127], a[180:183], v[4:7]\n\t" \
  "ds_read_b64_tr_b16 v[140:141], v156 offset:40960\n\t" \
  "v_alignbit_b32 v244, v244, v244, v248\n\t" \
  "v_mfma_f32_16x16x32_bf16 v[4:7], v[128:131], a[184:187], v[4:7]\n\t" \
  "v_exp_f32 v33, v33\n\t" \
  "v_mfma_f32_16x16x32_bf16 v[4:7], v[132:135], a[188:191], v[4:7]\n\t" \
  "ds_read_b64_tr_b16 v[142:143], v156 offset:45056\n\t" \
  "v_cvt_pk_bf16_f32 v114, v32, v33\n\t" \
  "v_mfma_f32_16x16x32_bf16 v[8:11], v[120:123], a[192:195], 0\n\t" \
  "v_exp_f32 v34, v34\n\t" \
  "v_mfma_f32_16x16x32_bf16 v[8:11], v[124:127], a[196:199], v[8:11]\n\t" \
  "ds_read_b64_tr_b16 v[144:145], v157 offset:32768\n\t" \
  "v_and_b32 v224, v244, v249\n\t" \
  "v_mfma_f32_16x16x32_bf16 v[8:11], v[128:131], a[200:203], v[8:11]\n\t" \
  "v_exp_f32 v35, v35\n\t" \
  "v_mfma_f32_16x16x32_bf16 v[8:11], v[132:135], a[204:207], v[8:11]\n\t" \
  "ds_read_b64_tr_b16 v[146:147], v157 offset:36864\n\t" \
  "v_cvt_pk_bf16_f32 v115, v34, v35\n\t" \
  "v_mfma_f32_16x16x32_bf16 v[12:15], v[120:123], a[208:211], 0\n\t" \
  "v_exp_f32 v36, v36\n\t" \
  "v_mfma_f32_16x16x32_bf16 v[12:15], v[124:127], a[212:215], v[12:15]\n\t" \
  "ds_read_b64_tr_b16 v[148:149], v157 offset:40960\n\t" \
  "v_mov_b32_dpp v245, v217 row_newbcast:4 row_mask:0xf bank_mask:0xf\n\t" \
  "v_mfma_f32_16x16x32_bf16 v[12:15], v[128:131], a[216:219], v[12:15]\n\t" \
  "v_exp_f32 v37, v37\n\t" \
  "v_mfma_f32_16x16x32_bf16 v[12:15], v[132:135], a[220:223], v[12:15]\n\t" \
  "ds_read_b64_tr_b16 v[150:151], v157 offset:45056\n\t" \
  "v_cvt_pk_bf16_f32 v118, v36, v37\n\t" \
  "v_mfma_f32_16x16x32_bf16 v[16:19], v[120:123], a[224:227], 0\n\t" \
  "s_waitcnt lgkmcnt(8)\n\t" \
  "v_exp_f32 v38, v38\n\t" \
  "v_mfma_f32_16x16x32_bf16 v[16:19], v[124:127], a[228:231], v[16:19]\n\t" \
  "ds_read_b64_tr_b16 v[236:237], v169 offset:0\n\t" \
  "v_alignbit_b32 v245, v245, v245, v248\n\t" \
  "v_mfma_f32_16x16x32_bf16 v[16:19], v[128:131], a[232:235], v[16:19]\n\t" \
  "s_waitcnt lgkmcnt(5)\n\t" \
  "v_exp_f32 v39, v39\n\t" \
  "v_mfma_f32_16x16x32_bf16 v[16:19], v[132:135], a[236:239], v[16:19]\n\t" \
  "v_cvt_pk_bf16_f32 v119, v38, v39\n\t" \
  "v_and_b32 v225, v245, v249\n\t" \
  "v_mfma_f32_16x16x32_bf16 v[192:195], v[232:235], v[228:231], v[192:195]\n\t" \
  "ds_read_b64_tr_b16 v[238:239], v169 offset:4096\n\t" \
  "v_mfma_f32_16x16x32_bf16 a[0:3], v[136:139], v[80:83], a[0:3]\n\t" \
  "v_exp_f32 v0, v0\n\t" \
  "v_mfma_f32_16x16x32_bf16 a[0:3], v[140:143], v[100:103], a[0:3]\n\t" \
  "ds_read_b128 v[120:123], v152 offset:4096\n\t" \
  "v_mfma_f32_16x16x32_bf16 a[4:7], v[136:139], v[84:87], a[4:7]\n\t" \
  "v_exp_f32 v1, v1\n\t" \
  "v_mfma_f32_16x16x32_bf16 a[4:7], v[140:143], v[104:107], a[4:7]\n\t" \
  "ds_read_b128 v[124:127], v153 offset:4096\n\t" \
  "v_cvt_pk_bf16_f32 v40, v0, v1\n\t" \
  "v_mfma_f32_16x16x32_bf16 a[8:11], v[136:139], v[88:91], a[8:11]\n\t" \
  "s_waitcnt lgkmcnt(4)\n\t" \
  "v_exp_f32 v2, v2\n\t" \
  "v_mfma_f32_16x16x32_bf16 a[8:11], v[140:143], v[108:111], a[8:11]\n\t" \
  "ds_read_b128 v[128:131], v154 offset:4096\n\t" \
  "v_mfma_f32_16x16x32_bf16 a[12:15], v[136:139], v[92:95], a[12:15]\n\t" \
  "s_add_u32 %[soff], %[soff], 12288\n\t" \
  "v_exp_f32 v3, v3\n\t" \
  "v_mfma_f32_16x16x32_bf16 a[12:15], v[140:143], v[112:115], a[12:15]\n\t" \
  "ds_read_b128 v[132:135], v155 offset:4096\n\t" \
  "v_cvt_pk_bf16_f32 v41, v2, v3\n\t" \
  "v_mfma_f32_16x16x32_bf16 a[16:19], v[136:139], v[96:99], a[16:19]\n\t" \
  "s_add_u32 m0, s72, 16384\n\t" \
  "v_exp_f32 v4, v4\n\t" \
  "v_mfma_f32_16x16x32_bf16 a[16:19], v[140:143], v[116:119], a[16:19]\n\t" \
  "buffer_load_dwordx4 v172, %[srd], %[soff] offen offset:0 lds\n\t" \
  "v_mfma_f32_16x16x32_bf16 a[20:23], v[144:147], v[80:83], a[20:23]\n\t" \
  "v_exp_f32 v5, v5\n\t" \
  "v_mfma_f32_16x16x32_bf16 a[20:23], v[148:151], v[100:103], a[20:23]\n\t" \
  "buffer_load_dwordx4 v172, %[srd], %[soff] offen offset:1024 lds\n\t" \
  "v_mfma_f32_16x16x32_bf16 a[24:27], v[144:147], v[84:87], a[24:27]\n\t" \
  "v_exp_f32 v6, v6\n\t" \
  "v_mfma_f32_16x16x32_bf16 a[24:27], v[148:151], v[104:107], a[24:27]\n\t" \
  "buffer_load_dwordx4 v172, %[srd], %[soff] offen offset:2048 lds\n\t" \
  "v_mfma_f32_16x16x32_bf16 a[28:31], v[144:147], v[88:91], a[28:31]\n\t" \
  "v_exp_f32 v7, v7\n\t" \
  "v_mfma_f32_16x16x32_bf16 a[28:31], v[148:151], v[108:111], a[28:31]\n\t" \
  "v_cvt_pk_bf16_f32 v44, v4, v5\n\t" \
  "v_cvt_pk_bf16_f32 v45, v6, v7\n\t" \
  "v_mfma_f32_16x16x32_bf16 a[32:35], v[144:147], v[92:95], a[32:35]\n\t" \
  "s_waitcnt lgkmcnt(4)\n\t" \
  "v_exp_f32 v8, v8\n\t" \
  "v_mfma_f32_16x16x32_bf16 a[32:35], v[148:151], v[112:115], a[32:35]\n\t" \
  "ds_read_b64_tr_b16 v[232:233], v170 offset:0\n\t" \
  "v_mfma_f32_16x16x32_bf16 a[36:39], v[144:147], v[96:99], a[36:39]\n\t" \
  "s_waitcnt lgkmcnt(1)\n\t" \
  "v_exp_f32 v9, v9\n\t" \
  "v_mfma_f32_16x16x32_bf16 a[36:39], v[148:151], v[116:119], a[36:39]\n\t" \
  "buffer_load_dwordx4 v172, %[srd], %[soff] offen offset:3072 lds\n\t" \
  "v_mfma_f32_16x16x32_bf16 v[196:199], v[236:239], v[228:231], v[196:199]\n\t" \
  "ds_read_b64_tr_b16 v[234:235], v170 offset:4096\n\t" \
  "v_cvt_pk_bf16_f32 v48, v8, v9\n\t" \
  "v_mfma_f32_16x16x32_bf16 v[20:23], v[120:123], a[160:163], 0\n\t" \
  "v_exp_f32 v10, v10\n\t" \
  "v_mfma_f32_16x16x32_bf16 v[20:23], v[124:127], a[164:167], v[20:23]\n\t" \
  "ds_read_b64_tr_b16 v[136:137], v158 offset:32768\n\t" \
  "v_mov_b32_dpp v244, v218 row_newbcast:4 row_mask:0xf bank_mask:0xf\n\t" \
  "v_mfma_f32_16x16x32_bf16 v[20:23], v[128:131], a[168:171], v[20:23]\n\t" \
  "v_exp_f32 v11, v11\n\t" \
  "v_mfma_f32_16x16x32_bf16 v[20:23], v[132:135], a[172:175], v[20:23]\n\t" \
  "ds_read_b64_tr_b16 v[138:139], v158 offset:36864\n\t" \
  "v_cvt_pk_bf16_f32 v49, v10, v11\n\t" \
  "v_mfma_f32_16x16x32_bf16 v[24:27], v[120:123], a[176:179], 0\n\t" \
  "v_exp_f32 v12, v12\n\t" \
  "v_mfma_f32_16x16x32_bf16 v[24:27], v[124:127], a[180:183], v[24:27]\n\t" \
  "ds_read_b64_tr_b16 v[140:141], v158 offset:40960\n\t" \
  "v_alignbit_b32 v244, v244, v244, v248\n\t" \
  "v_mfma_f32_16x16x32_bf16 v[24:27], v[128:131], a[184:187], v[24:27]\n\t" \
  "v_exp_f32 v13, v13\n\t" \
  "v_mfma_f32_16x16x32_bf16 v[24:27], v[132:135], a[188:191], v[24:27]\n\t" \
  "ds_read_b64_tr_b16 v[142:143], v158 offset:45056\n\t" \
  "v_cvt_pk_bf16_f32 v52, v12, v13\n\t" \
  "v_mfma_f32_16x16x32_bf16 v[28:31], v[120:123], a[192:195], 0\n\t" \
  "v_exp_f32 v14, v14\n\t" \
  "v_mfma_f32_16x16x32_bf16 v[28:31], v[124:127], a[196:199], v[28:31]\n\t" \
  "ds_read_b64_tr_b16 v[144:145], v159 offset:32768\n\t" \
  "v_and_b32 v226, v244, v249\n\t" \
  "v_mfma_f32_16x16x32_bf16 v[28:31], v[128:131], a[200:203], v[28:31]\n\t" \
  "v_exp_f32 v15, v15\n\t" \
  "v_mfma_f32_16x16x32_bf16 v[28:31], v[132:135], a[204:207], v[28:31]\n\t" \
  "ds_read_b64_tr_b16 v[146:147], v159 offset:36864\n\t" \
  "v_cvt_pk_bf16_f32 v53, v14, v15\n\t" \
  "v_mfma_f32_16x16x32_bf16 v[32:35], v[120:123], a[208:211], 0\n\t" \
  "v_exp_f32 v16, v16\n\t" \
  "v_mfma_f32_16x16x32_bf16 v[32:35], v[124:127], a[212:215], v[32:35]\n\t" \
  "ds_read_b64_tr_b16 v[148:149], v159 offset:40960\n\t" \
  "v_mov_b32_dpp v245, v219 row_newbcast:4 row_mask:0xf bank_mask:0xf\n\t" \
  "v_mfma_f32_16x16x32_bf16 v[32:35], v[128:131], a[216:219], v[32:35]\n\t" \
  "v_exp_f32 v17, v17\n\t" \
  "v_mfma_f32_16x16x32_bf16 v[32:35], v[132:135], a[220:223], v[32:35]\n\t" \
  "ds_read_b64_tr_b16 v[150:151], v159 offset:45056\n\t" \
  "v_cvt_pk_bf16_f32 v56, v16, v17\n\t" \
  "v_mfma_f32_16x16x32_bf16 v[36:39], v[120:123], a[224:227], 0\n\t" \
  "s_waitcnt lgkmcnt(8)\n\t" \
  "v_exp_f32 v18, v18\n\t" \
  "v_mfma_f32_16x16x32_bf16 v[36:39], v[124:127], a[228:231], v[36:39]\n\t" \
  "ds_read_b64_tr_b16 v[236:237], v171 offset:0\n\t" \
  "v_alignbit_b32 v245, v245, v245, v248\n\t" \
  "v_mfma_f32_16x16x32_bf16 v[36:39], v[128:131], a[232:235], v[36:39]\n\t" \
  "s_waitcnt lgkmcnt(5)\n\t" \
  "v_exp_f32 v19, v19\n\t" \
  "v_mfma_f32_16x16x32_bf16 v[36:39], v[132:135], a[236:239], v[36:39]\n\t" \
  "v_cvt_pk_bf16_f32 v57, v18, v19\n\t" \
  "v_and_b32 v227, v245, v249\n\t" \
  "v_mfma_f32_16x16x32_bf16 v[200:203], v[232:235], v[228:231], v[200:203]\n\t" \
  "ds_read_b64_tr_b16 v[238:239], v171 offset:4096\n\t" \
  "v_mfma_f32_16x16x32_bf16 a[40:43], v[136:139], v[80:83], a[40:43]\n\t" \
  "v_exp_f32 v20, v20\n\t" \
  "v_mfma_f32_16x16x32_bf16 a[40:43], v[140:143], v[100:103], a[40:43]\n\t" \
  "ds_read_b128 v[120:123], v152 offset:8192\n\t" \
  "v_xor_b32_dpp v240, v208, v247 row_newbcast:4 row_mask:0xf bank_mask:0xf\n\t" \
  "v_mfma_f32_16x16x32_bf16 a[44:47], v[136:139], v[84:87], a[44:47]\n\t" \
  "v_exp_f32 v21, v21\n\t" \
  "v_mfma_f32_16x16x32_bf16 a[44:47], v[140:143], v[104:107], a[44:47]\n\t" \
  "ds_read_b128 v[124:127], v153 offset:8192\n\t" \
  "v_cvt_pk_bf16_f32 v42, v20, v21\n\t" \
  "v_mfma_f32_16x16x32_bf16 a[48:51], v[136:139], v[88:91], a[48:51]\n\t" \
  "s_waitcnt lgkmcnt(4)\n\t" \
  "v_exp_f32 v22, v22\n\t" \
  "v_mfma_f32_16x16x32_bf16 a[48:51], v[140:143], v[108:111], a[48:51]\n\t" \
  "ds_read_b128 v[128:131], v154 offset:8192\n\t" \
  "v_xor_b32_dpp v241, v209, v247 row_newbcast:4 row_mask:0xf bank_mask:0xf\n\t" \
  "v_mfma_f32_16x16x32_bf16 a[52:55], v[136:139], v[92:95], a[52:55]\n\t" \
  "s_add_u32 %[soff], %[soff], 4096\n\t" \
  "v_exp_f32 v23, v23\n\t" \
  "v_mfma_f32_16x16x32_bf16 a[52:55], v[140:143], v[112:115], a[52:55]\n\t" \
  "ds_read_b128 v[132:135], v155 offset:8192\n\t" \
  "v_cvt_pk_bf16_f32 v43, v22, v23\n\t" \
  "v_mfma_f32_16x16x32_bf16 a[56:59], v[136:139], v[96:99], a[56:59]\n\t" \
  "s_add_u32 m0, s72, 20480\n\t" \
  "v_exp_f32 v24, v24\n\t" \
  "v_mfma_f32_16x16x32_bf16 a[56:59], v[140:143], v[116:119], a[56:59]\n\t" \
  "buffer_load_dwordx4 v172, %[srd], %[soff] offen offset:0 lds\n\t" \
  "v_mfma_f32_16x16x32_bf16 a[60:63], v[144:147], v[80:83], a[60:63]\n\t" \
  "v_exp_f32 v25, v25\n\t" \
  "v_mfma_f32_16x16x32_bf16 a[60:63], v[148:151], v[100:103], a[60:63]\n\t" \
  "buffer_load_dwordx4 v172, %[srd], %[soff] offen offset:1024 lds\n\t" \
  "v_mfma_f32_16x16x32_bf16 a[64:67], v[144:147], v[84:87], a[64:67]\n\t" \
  "v_exp_f32 v26, v26\n\t" \
  "v_mfma_f32_16x16x32_bf16 a[64:67], v[148:151], v[104:107], a[64:67]\n\t" \
  "buffer_load_dwordx4 v172, %[srd], %[soff] offen offset:2048 lds\n\t" \
  "v_mfma_f32_16x16x32_bf16 a[68:71], v[144:147], v[88:91], a[68:71]\n\t" \
  "s_waitcnt vmcnt(7)\n\t" \
  "v_exp_f32 v27, v27\n\t" \
  "v_mfma_f32_16x16x32_bf16 a[68:71], v[148:151], v[108:111], a[68:71]\n\t" \
  "v_cvt_pk_bf16_f32 v46, v24, v25\n\t" \
  "v_cvt_pk_bf16_f32 v47, v26, v27\n\t" \
  "v_mfma_f32_16x16x32_bf16 a[72:75], v[144:147], v[92:95], a[72:75]\n\t" \
  "s_waitcnt lgkmcnt(4)\n\t" \
  "v_exp_f32 v28, v28\n\t" \
  "v_mfma_f32_16x16x32_bf16 a[72:75], v[148:151], v[112:115], a[72:75]\n\t" \
  "ds_read_b64_tr_b16 v[232:233], v164 offset:8192\n\t" \
  "v_xor_b32_dpp v242, v210, v247 row_newbcast:4 row_mask:0xf bank_mask:0xf\n\t" \
  "v_mfma_f32_16x16x32_bf16 a[76:79], v[144:147], v[96:99], a[76:79]\n\t" \
  "s_waitcnt lgkmcnt(1)\n\t" \
  "v_exp_f32 v29, v29\n\t" \
  "v_mfma_f32_16x16x32_bf16 a[76:79], v[148:151], v[116:119], a[76:79]\n\t" \
  "buffer_load_dwordx4 v172, %[srd], %[soff] offen offset:3072 lds\n\t" \
  "v_mfma_f32_16x16x32_bf16 v[204:207], v[236:239], v[228:231], v[204:207]\n\t" \
  "ds_read_b64_tr_b16 v[234:235], v164 offset:12288\n\t" \
  "v_cvt_pk_bf16_f32 v50, v28, v29\n\t" \
  "v_mfma_f32_16x16x32_bf16 v[0:3], v[120:123], a[160:163], 0\n\t" \
  "v_exp_f32 v30, v30\n\t" \
  "v_mfma_f32_16x16x32_bf16 v[0:3], v[124:127], a[164:167], v[0:3]\n\t" \
  "ds_read_b64_tr_b16 v[136:137], v160 offset:32768\n\t" \
  "v_xor_b32_dpp v243, v211, v247 row_newbcast:4 row_mask:0xf bank_mask:0xf\n\t" \
  "v_mfma_f32_16x16x32_bf16 v[0:3], v[128:131], a[168:171], v[0:3]\n\t" \
  "v_exp_f32 v31, v31\n\t" \
  "v_mfma_f32_16x16x32_bf16 v[0:3], v[132:135], a[172:175], v[0:3]\n\t" \
  "ds_read_b64_tr_b16 v[138:139], v160 offset:36864\n\t" \
  "v_cvt_pk_bf16_f32 v51, v30, v31\n\t" \
  "v_mfma_f32_16x16x32_bf16 v[4:7], v[120:123], a[176:179], 0\n\t" \
  "v_exp_f32 v32, v32\n\t" \
  "v_mfma_f32_16x16x32_bf16 v[4:7], v[124:127], a[180:183], v[4:7]\n\t" \
  "ds_read_b64_tr_b16 v[140:141], v160 offset:40960\n\t" \
  "v_mfma_f32_16x16x32_bf16 v[4:7], v[128:131], a[184:187], v[4:7]\n\t" \
  "v_exp_f32 v33, v33\n\t" \
  "v_mfma_f32_16x16x32_bf16 v[4:7], v[132:135], a[188:191], v[4:7]\n\t" \
  "ds_read_b64_tr_b16 v[142:143], v160 offset:45056\n\t" \
  "v_cvt_pk_bf16_f32 v54, v32, v33\n\t" \
  "v_mfma_f32_16x16x32_bf16 v[8:11], v[120:123], a[192:195], 0\n\t" \
  "v_exp_f32 v34, v34\n\t" \
  "v_mfma_f32_16x16x32_bf16 v[8:11], v[124:127], a[196:199], v[8:11]\n\t" \
  "ds_read_b64_tr_b16 v[144:145], v161 offset:32768\n\t" \
  "v_mfma_f32_16x16x32_bf16 v[8:11], v[128:131], a[200:203], v[8:11]\n\t" \
  "v_exp_f32 v35, v35\n\t" \
  "v_mfma_f32_16x16x32_bf16 v[8:11], v[132:135], a[204:207], v[8:11]\n\t" \
  "ds_read_b64_tr_b16 v[146:147], v161 offset:36864\n\t" \
  "v_cvt_pk_bf16_f32 v55, v34, v35\n\t" \
  "v_mfma_f32_16x16x32_bf16 v[12:15], v[120:123], a[208:211], 0\n\t" \
  "v_exp_f32 v36, v36\n\t" \
  "v_mfma_f32_16x16x32_bf16 v[12:15], v[124:127], a[212:215], v[12:15]\n\t" \
  "ds_read_b64_tr_b16 v[148:149], v161 offset:40960\n\t" \
  "v_mfma_f32_16x16x32_bf16 v[12:15], v[128:131], a[216:219], v[12:15]\n\t" \
  "v_exp_f32 v37, v37\n\t" \
  "v_mfma_f32_16x16x32_bf16 v[12:15], v[132:135], a[220:223], v[12:15]\n\t" \
  "ds_read_b64_tr_b16 v[150:151], v161 offset:45056\n\t" \
  "v_cvt_pk_bf16_f32 v58, v36, v37\n\t" \
  "v_mfma_f32_16x16x32_bf16 v[16:19], v[120:123], a[224:227], 0\n\t" \
  "s_waitcnt lgkmcnt(8)\n\t" \
  "v_exp_f32 v38, v38\n\t" \
  "v_mfma_f32_16x16x32_bf16 v[16:19], v[124:127], a[228:231], v[16:19]\n\t" \
  "ds_read_b64_tr_b16 v[236:237], v165 offset:8192\n\t" \
  "v_mfma_f32_16x16x32_bf16 v[16:19], v[128:131], a[232:235], v[16:19]\n\t" \
  "s_waitcnt lgkmcnt(5)\n\t" \
  "v_exp_f32 v39, v39\n\t" \
  "v_mfma_f32_16x16x32_bf16 v[16:19], v[132:135], a[236:239], v[16:19]\n\t" \
  "v_cvt_pk_bf16_f32 v59, v38, v39\n\t" \
  "v_mfma_f32_16x16x32_bf16 v[176:179], v[232:235], v[220:223], v[176:179]\n\t" \
  "ds_read_b64_tr_b16 v[238:239], v165 offset:12288\n\t" \
  "v_mfma_f32_16x16x32_bf16 a[80:83], v[136:139], v[80:83], a[80:83]\n\t" \
  "v_exp_f32 v0, v0\n\t" \
  "v_mfma_f32_16x16x32_bf16 a[80:83], v[140:143], v[100:103], a[80:83]\n\t" \
  "ds_read_b128 v[120:123], v152 offset:12288\n\t" \
  "v_mfma_f32_16x16x32_bf16 a[84:87], v[136:139], v[84:87], a[84:87]\n\t" \
  "v_exp_f32 v1, v1\n\t" \
  "v_mfma_f32_16x16x32_bf16 a[84:87], v[140:143], v[104:107], a[84:87]\n\t" \
  "ds_read_b128 v[124:127], v153 offset:12288\n\t" \
  "v_cvt_pk_bf16_f32 v60, v0, v1\n\t" \
  "v_mfma_f32_16x16x32_bf16 a[88:91], v[136:139], v[88:91], a[88:91]\n\t" \
  "s_waitcnt lgkmcnt(4)\n\t" \
  "v_exp_f32 v2, v2\n\t" \
  "v_mfma_f32_16x16x32_bf16 a[88:91], v[140:143], v[108:111], a[88:91]\n\t" \
  "ds_read_b128 v[128:131], v154 offset:12288\n\t" \
  "v_mfma_f32_16x16x32_bf16 a[92:95], v[136:139], v[92:95], a[92:95]\n\t" \
  "v_exp_f32 v3, v3\n\t" \
  "v_mfma_f32_16x16x32_bf16 a[92:95], v[140:143], v[112:115], a[92:95]\n\t" \
  "ds_read_b128 v[132:135], v155 offset:12288\n\t" \
  "v_cvt_pk_bf16_f32 v61, v2, v3\n\t" \
  "v_mfma_f32_16x16x32_bf16 a[96:99], v[136:139], v[96:99], a[96:99]\n\t" \
  "s_add_u32 m0, s74, 0\n\t" \
  "v_exp_f32 v4, v4\n\t" \
  "v_mfma_f32_16x16x32_bf16 a[96:99], v[140:143], v[116:119], a[96:99]\n\t" \
  "buffer_load_dwordx4 v240, %[srdg], s84 offen offset:0 lds\n\t" \
  "v_mfma_f32_16x16x32_bf16 a[100:103], v[144:147], v[80:83], a[100:103]\n\t" \
  "v_exp_f32 v5, v5\n\t" \
  "v_mfma_f32_16x16x32_bf16 a[100:103], v[148:151], v[100:103], a[100:103]\n\t" \
  "buffer_load_dwordx4 v241, %[srdg], s85 offen offset:1024 lds\n\t" \
  "v_mfma_f32_16x16x32_bf16 a[104:107], v[144:147], v[84:87], a[104:107]\n\t" \
  "v_exp_f32 v6, v6\n\t" \
  "v_mfma_f32_16x16x32_bf16 a[104:107], v[148:151], v[104:107], a[104:107]\n\t" \
  "buffer_load_dwordx4 v242, %[srdg], s86 offen offset:2048 lds\n\t" \
  "v_mfma_f32_16x16x32_bf16 a[108:111], v[144:147], v[88:91], a[108:111]\n\t" \
  "v_exp_f32 v7, v7\n\t" \
  "v_mfma_f32_16x16x32_bf16 a[108:111], v[148:151], v[108:111], a[108:111]\n\t" \
  "v_cvt_pk_bf16_f32 v64, v4, v5\n\t" \
  "v_cvt_pk_bf16_f32 v65, v6, v7\n\t" \
  "v_mfma_f32_16x16x32_bf16 a[112:115], v[144:147], v[92:95], a[112:115]\n\t" \
  "s_waitcnt lgkmcnt(4)\n\t" \
  "v_exp_f32 v8, v8\n\t" \
  "v_mfma_f32_16x16x32_bf16 a[112:115], v[148:151], v[112:115], a[112:115]\n\t" \
  "ds_read_b64_tr_b16 v[232:233], v166 offset:8192\n\t" \
  "v_xor_b32_dpp v240, v212, v247 row_newbcast:4 row_mask:0xf bank_mask:0xf\n\t" \
  "v_mfma_f32_16x16x32_bf16 a[116:119], v[144:147], v[96:99], a[116:119]\n\t" \
  "s_waitcnt lgkmcnt(1)\n\t" \
  "v_exp_f32 v9, v9\n\t" \
  "v_mfma_f32_16x16x32_bf16 a[116:119], v[148:151], v[116:119], a[116:119]\n\t" \
  "buffer_load_dwordx4 v243, %[srdg], s87 offen offset:3072 lds\n\t" \
  "v_mfma_f32_16x16x32_bf16 v[180:183], v[236:239], v[220:223], v[180:183]\n\t" \
  "ds_read_b64_tr_b16 v[234:235], v166 offset:12288\n\t" \
  "v_cvt_pk_bf16_f32 v68, v8, v9\n\t" \
  "v_mfma_f32_16x16x32_bf16 v[20:23], v[120:123], a[160:163], 0\n\t" \
  "v_exp_f32 v10, v10\n\t" \
  "v_mfma_f32_16x16x32_bf16 v[20:23], v[124:127], a[164:167], v[20:23]\n\t" \
  "ds_read_b64_tr_b16 v[136:137], v162 offset:32768\n\t" \
  "v_xor_b32_dpp v241, v213, v247 row_newbcast:4 row_mask:0xf bank_mask:0xf\n\t" \
  "v_mfma_f32_16x16x32_bf16 v[20:23], v[128:131], a[168:171], v[20:23]\n\t" \
  "v_exp_f32 v11, v11\n\t" \
  "v_mfma_f32_16x16x32_bf16 v[20:23], v[132:135], a[172:175], v[20:23]\n\t" \
  "ds_read_b64_tr_b16 v[138:139], v162 offset:36864\n\t" \
  "v_cvt_pk_bf16_f32 v69, v10, v11\n\t" \
  "v_mfma_f32_16x16x32_bf16 v[24:27], v[120:123], a[176:179], 0\n\t" \
  "v_exp_f32 v12, v12\n\t" \
  "v_mfma_f32_16x16x32_bf16 v[24:27], v[124:127], a[180:183], v[24:27]\n\t" \
  "ds_read_b64_tr_b16 v[140:141], v162 offset:40960\n\t" \
  "v_xor_b32_dpp v242, v214, v247 row_newbcast:4 row_mask:0xf bank_mask:0xf\n\t" \
  "v_mfma_f32_16x16x32_bf16 v[24:27], v[128:131], a[184:187], v[24:27]\n\t" \
  "v_exp_f32 v13, v13\n\t" \
  "v_mfma_f32_16x16x32_bf16 v[24:27], v[132:135], a[188:191], v[24:27]\n\t" \
  "ds_read_b64_tr_b16 v[142:143], v162 offset:45056\n\t" \
  "v_cvt_pk_bf16_f32 v72, v12, v13\n\t" \
  "v_mfma_f32_16x16x32_bf16 v[28:31], v[120:123], a[192:195], 0\n\t" \
  "v_exp_f32 v14, v14\n\t" \
  "v_mfma_f32_16x16x32_bf16 v[28:31], v[124:127], a[196:199], v[28:31]\n\t" \
  "ds_read_b64_tr_b16 v[144:145], v163 offset:32768\n\t" \
  "v_xor_b32_dpp v243, v215, v247 row_newbcast:4 row_mask:0xf bank_mask:0xf\n\t" \
  "v_mfma_f32_16x16x32_bf16 v[28:31], v[128:131], a[200:203], v[28:31]\n\t" \
  "v_exp_f32 v15, v15\n\t" \
  "v_mfma_f32_16x16x32_bf16 v[28:31], v[132:135], a[204:207], v[28:31]\n\t" \
  "ds_read_b64_tr_b16 v[146:147], v163 offset:36864\n\t" \
  "v_cvt_pk_bf16_f32 v73, v14, v15\n\t" \
  "v_mfma_f32_16x16x32_bf16 v[32:35], v[120:123], a[208:211], 0\n\t" \
  "v_exp_f32 v16, v16\n\t" \
  "v_mfma_f32_16x16x32_bf16 v[32:35], v[124:127], a[212:215], v[32:35]\n\t" \
  "ds_read_b64_tr_b16 v[148:149], v163 offset:40960\n\t" \
  "v_mfma_f32_16x16x32_bf16 v[32:35], v[128:131], a[216:219], v[32:35]\n\t" \
  "v_exp_f32 v17, v17\n\t" \
  "v_mfma_f32_16x16x32_bf16 v[32:35], v[132:135], a[220:223], v[32:35]\n\t" \
  "ds_read_b64_tr_b16 v[150:151], v163 offset:45056\n\t" \
  "v_cvt_pk_bf16_f32 v76, v16, v17\n\t" \
  "v_mfma_f32_16x16x32_bf16 v[36:39], v[120:123], a[224:227], 0\n\t" \
  "s_waitcnt lgkmcnt(8)\n\t" \
  "v_exp_f32 v18, v18\n\t" \
  "v_mfma_f32_16x16x32_bf16 v[36:39], v[124:127], a[228:231], v[36:39]\n\t" \
  "ds_read_b64_tr_b16 v[236:237], v167 offset:8192\n\t" \
  "v_mfma_f32_16x16x32_bf16 v[36:39], v[128:131], a[232:235], v[36:39]\n\t" \
  "s_waitcnt lgkmcnt(5)\n\t" \
  "v_exp_f32 v19, v19\n\t" \
  "v_mfma_f32_16x16x32_bf16 v[36:39], v[132:135], a[236:239], v[36:39]\n\t" \
  "v_cvt_pk_bf16_f32 v77, v18, v19\n\t" \
  "v_mfma_f32_16x16x32_bf16 v[184:187], v[232:235], v[220:223], v[184:187]\n\t" \
  "ds_read_b64_tr_b16 v[238:239], v167 offset:12288\n\t" \
  "v_mfma_f32_16x16x32_bf16 a[120:123], v[136:139], v[80:83], a[120:123]\n\t" \
  "s_add_u32 m0, s74, 4096\n\t" \
  "v_exp_f32 v20, v20\n\t" \
  "v_mfma_f32_16x16x32_bf16 a[120:123], v[140:143], v[100:103], a[120:123]\n\t" \
  "buffer_load_dwordx4 v240, %[srdg], s84 offen offset:0 lds\n\t" \
  "v_mfma_f32_16x16x32_bf16 a[124:127], v[136:139], v[84:87], a[124:127]\n\t" \
  "v_exp_f32 v21, v21\n\t" \
  "v_mfma_f32_16x16x32_bf16 a[124:127], v[140:143], v[104:107], a[124:127]\n\t" \
  "buffer_load_dwordx4 v241, %[srdg], s85 offen offset:1024 lds\n\t" \
  "v_mfma_f32_16x16x32_bf16 a[128:131], v[136:139], v[88:91], a[128:131]\n\t" \
  "s_waitcnt lgkmcnt(2)\n\t" \
  "v_exp_f32 v22, v22\n\t" \
  "v_mfma_f32_16x16x32_bf16 a[128:131], v[140:143], v[108:111], a[128:131]\n\t" \
  "buffer_load_dwordx4 v242, %[srdg], s86 offen offset:2048 lds\n\t" \
  "v_mfma_f32_16x16x32_bf16 a[132:135], v[136:139], v[92:95], a[132:135]\n\t" \
  "s_waitcnt vmcnt(7)\n\t" \
  "s_barrier\n\t" \
  "v_exp_f32 v23, v23\n\t" \
  "v_mfma_f32_16x16x32_bf16 a[132:135], v[140:143], v[112:115], a[132:135]\n\t" \
  "ds_read_b128 v[120:123], v152 offset:16384\n\t" \
  "v_cvt_pk_bf16_f32 v62, v20, v21\n\t" \
  "v_mfma_f32_16x16x32_bf16 a[136:139], v[136:139], v[96:99], a[136:139]\n\t" \
  "v_exp_f32 v24, v24\n\t" \
  "v_mfma_f32_16x16x32_bf16 a[136:139], v[140:143], v[116:119], a[136:139]\n\t" \
  "ds_read_b128 v[124:127], v153 offset:16384\n\t" \
  "v_cvt_pk_bf16_f32 v63, v22, v23\n\t" \
  "v_mfma_f32_16x16x32_bf16 a[140:143], v[144:147], v[80:83], a[140:143]\n\t" \
  "v_exp_f32 v25, v25\n\t" \
  "v_mfma_f32_16x16x32_bf16 a[140:143], v[148:151], v[100:103], a[140:143]\n\t" \
  "ds_read_b128 v[128:131], v154 offset:16384\n\t" \
  "v_cvt_pk_bf16_f32 v66, v24, v25\n\t" \
  "v_mfma_f32_16x16x32_bf16 a[144:147], v[144:147], v[84:87], a[144:147]\n\t" \
  "v_exp_f32 v26, v26\n\t" \
  "v_mfma_f32_16x16x32_bf16 a[144:147], v[148:151], v[104:107], a[144:147]\n\t" \
  "ds_read_b128 v[132:135], v155 offset:16384\n\t" \
  "v_mfma_f32_16x16x32_bf16 a[148:151], v[144:147], v[88:91], a[148:151]\n\t" \
  "v_exp_f32 v27, v27\n\t" \
  "v_mfma_f32_16x16x32_bf16 a[148:151], v[148:151], v[108:111], a[148:151]\n\t" \
  "v_cvt_pk_bf16_f32 v67, v26, v27\n\t" \
  "v_mfma_f32_16x16x32_bf16 a[152:155], v[144:147], v[92:95], a[152:155]\n\t" \
  "s_waitcnt lgkmcnt(4)\n\t" \
  "v_exp_f32 v28, v28\n\t" \
  "v_mfma_f32_16x16x32_bf16 a[152:155], v[148:151], v[112:115], a[152:155]\n\t" \
  "ds_read_b64_tr_b16 v[232:233], v168 offset:8192\n\t" \
  "v_mfma_f32_16x16x32_bf16 a[156:159], v[144:147], v[96:99], a[156:159]\n\t" \
  "s_waitcnt lgkmcnt(1)\n\t" \
  "v_exp_f32 v29, v29\n\t" \
  "v_mfma_f32_16x16x32_bf16 a[156:159], v[148:151], v[116:119], a[156:159]\n\t" \
  "buffer_load_dwordx4 v243, %[srdg], s87 offen offset:3072 lds\n\t" \
  "v_mfma_f32_16x16x32_bf16 v[188:191], v[236:239], v[220:223], v[188:191]\n\t" \
  "ds_read_b64_tr_b16 v[234:235], v168 offset:12288\n\t" \
  "v_cvt_pk_bf16_f32 v70, v28, v29\n\t" \
  "v_mfma_f32_16x16x32_bf16 v[0:3], v[120:123], a[160:163], 0\n\t" \
  "v_exp_f32 v30, v30\n\t" \
  "v_mfma_f32_16x16x32_bf16 v[0:3], v[124:127], a[164:167], v[0:3]\n\t" \
  "ds_read_b64_tr_b16 v[136:137], v156 offset:0\n\t" \
  "v_mov_b32_dpp v244, v216 row_newbcast:5 row_mask:0xf bank_mask:0xf\n\t" \
  "v_mfma_f32_16x16x32_bf16 v[0:3], v[128:131], a[168:171], v[0:3]\n\t" \
  "v_exp_f32 v31, v31\n\t" \
  "v_mfma_f32_16x16x32_bf16 v[0:3], v[132:135], a[172:175], v[0:3]\n\t" \
  "ds_read_b64_tr_b16 v[138:139], v156 offset:4096\n\t" \
  "v_cvt_pk_bf16_f32 v71, v30, v31\n\t" \
  "v_mfma_f32_16x16x32_bf16 v[4:7], v[120:123], a[176:179], 0\n\t" \
  "v_exp_f32 v32, v32\n\t" \
  "v_mfma_f32_16x16x32_bf16 v[4:7], v[124:127], a[180:183], v[4:7]\n\t" \
  "ds_read_b64_tr_b16 v[140:141], v156 offset:8192\n\t" \
  "v_alignbit_b32 v244, v244, v244, v248\n\t" \
  "v_mfma_f32_16x16x32_bf16 v[4:7], v[128:131], a[184:187], v[4:7]\n\t" \
  "v_exp_f32 v33, v33\n\t" \
  "v_mfma_f32_16x16x32_bf16 v[4:7], v[132:135], a[188:191], v[4:7]\n\t" \
  "ds_read_b64_tr_b16 v[142:143], v156 offset:12288\n\t" \
  "v_cvt_pk_bf16_f32 v74, v32, v33\n\t" \
  "v_mfma_f32_16x16x32_bf16 v[8:11], v[120:123], a[192:195], 0\n\t" \
  "v_exp_f32 v34, v34\n\t" \
  "v_mfma_f32_16x16x32_bf16 v[8:11], v[124:127], a[196:199], v[8:11]\n\t" \
  "ds_read_b64_tr_b16 v[144:145], v157 offset:0\n\t" \
  "v_and_b32 v228, v244, v249\n\t" \
  "v_mfma_f32_16x16x32_bf16 v[8:11], v[128:131], a[200:203], v[8:11]\n\t" \
  "v_exp_f32 v35, v35\n\t" \
  "v_mfma_f32_16x16x32_bf16 v[8:11], v[132:135], a[204:207], v[8:11]\n\t" \
  "ds_read_b64_tr_b16 v[146:147], v157 offset:4096\n\t" \
  "v_cvt_pk_bf16_f32 v75, v34, v35\n\t" \
  "v_mfma_f32_16x16x32_bf16 v[12:15], v[120:123], a[208:211], 0\n\t" \
  "v_exp_f32 v36, v36\n\t" \
  "v_mfma_f32_16x16x32_bf16 v[12:15], v[124:127], a[212:215], v[12:15]\n\t" \
  "ds_read_b64_tr_b16 v[148:149], v157 offset:8192\n\t" \
  "v_mov_b32_dpp v245, v217 row_newbcast:5 row_mask:0xf bank_mask:0xf\n\t" \
  "v_mfma_f32_16x16x32_bf16 v[12:15], v[128:131], a[216:219], v[12:15]\n\t" \
  "v_exp_f32 v37, v37\n\t" \
  "v_mfma_f32_16x16x32_bf16 v[12:15], v[132:135], a[220:223], v[12:15]\n\t" \
  "ds_read_b64_tr_b16 v[150:151], v157 offset:12288\n\t" \
  "v_cvt_pk_bf16_f32 v78, v36, v37\n\t" \
  "v_mfma_f32_16x16x32_bf16 v[16:19], v[120:123], a[224:227], 0\n\t" \
  "s_waitcnt lgkmcnt(8)\n\t" \
  "v_exp_f32 v38, v38\n\t" \
  "v_mfma_f32_16x16x32_bf16 v[16:19], v[124:127], a[228:231], v[16:19]\n\t" \
  "ds_read_b64_tr_b16 v[236:237], v169 offset:8192\n\t" \
  "v_alignbit_b32 v245, v245, v245, v248\n\t" \
  "v_mfma_f32_16x16x32_bf16 v[16:19], v[128:131], a[232:235], v[16:19]\n\t" \
  "s_waitcnt lgkmcnt(5)\n\t" \
  "v_exp_f32 v39, v39\n\t" \
  "v_mfma_f32_16x16x32_bf16 v[16:19], v[132:135], a[236:239], v[16:19]\n\t" \
  "v_cvt_pk_bf16_f32 v79, v38, v39\n\t" \
  "v_and_b32 v229, v245, v249\n\t" \
  "v_mfma_f32_16x16x32_bf16 v[192:195], v[232:235], v[220:223], v[192:195]\n\t" \
  "ds_read_b64_tr_b16 v[238:239], v169 offset:12288\n\t" \
  "v_mfma_f32_16x16x32_bf16 a[0:3], v[136:139], v[40:43], a[0:3]\n\t" \
  "v_exp_f32 v0, v0\n\t" \
  "v_mfma_f32_16x16x32_bf16 a[0:3], v[140:143], v[60:63], a[0:3]\n\t" \
  "ds_read_b128 v[120:123], v152 offset:20480\n\t" \
  "v_mfma_f32_16x16x32_bf16 a[4:7], v[136:139], v[44:47], a[4:7]\n\t" \
  "v_exp_f32 v1, v1\n\t" \
  "v_mfma_f32_16x16x32_bf16 a[4:7], v[140:143], v[64:67], a[4:7]\n\t" \
  "ds_read_b128 v[124:127], v153 offset:20480\n\t" \
  "v_cvt_pk_bf16_f32 v80, v0, v1\n\t" \
  "v_mfma_f32_16x16x32_bf16 a[8:11], v[136:139], v[48:51], a[8:11]\n\t" \
  "s_waitcnt lgkmcnt(4)\n\t" \
  "v_exp_f32 v2, v2\n\t" \
  "v_mfma_f32_16x16x32_bf16 a[8:11], v[140:143], v[68:71], a[8:11]\n\t" \
  "ds_read_b128 v[128:131], v154 offset:20480\n\t" \
  "v_mfma_f32_16x16x32_bf16 a[12:15], v[136:139], v[52:55], a[12:15]\n\t" \
  "s_add_u32 %[soff], %[soff], 12288\n\t" \
  "v_exp_f32 v3, v3\n\t" \
  "v_mfma_f32_16x16x32_bf16 a[12:15], v[140:143], v[72:75], a[12:15]\n\t" \
  "ds_read_b128 v[132:135], v155 offset:20480\n\t" \
  "v_cvt_pk_bf16_f32 v81, v2, v3\n\t" \
  "v_mfma_f32_16x16x32_bf16 a[16:19], v[136:139], v[56:59], a[16:19]\n\t" \
  "s_add_u32 m0, s72, 32768\n\t" \
  "v_exp_f32 v4, v4\n\t" \
  "v_mfma_f32_16x16x32_bf16 a[16:19], v[140:143], v[76:79], a[16:19]\n\t" \
  "buffer_load_dwordx4 v172, %[srd], %[soff] offen offset:0 lds\n\t" \
  "v_mfma_f32_16x16x32_bf16 a[20:23], v[144:147], v[40:43], a[20:23]\n\t" \
  "v_exp_f32 v5, v5\n\t" \
  "v_mfma_f32_16x16x32_bf16 a[20:23], v[148:151], v[60:63], a[20:23]\n\t" \
  "buffer_load_dwordx4 v172, %[srd], %[soff] offen offset:1024 lds\n\t" \
  "v_mfma_f32_16x16x32_bf16 a[24:27], v[144:147], v[44:47], a[24:27]\n\t" \
  "v_exp_f32 v6, v6\n\t" \
  "v_mfma_f32_16x16x32_bf16 a[24:27], v[148:151], v[64:67], a[24:27]\n\t" \
  "buffer_load_dwordx4 v172, %[srd], %[soff] offen offset:2048 lds\n\t" \
  "v_mfma_f32_16x16x32_bf16 a[28:31], v[144:147], v[48:51], a[28:31]\n\t" \
  "v_exp_f32 v7, v7\n\t" \
  "v_mfma_f32_16x16x32_bf16 a[28:31], v[148:151], v[68:71], a[28:31]\n\t" \
  "v_cvt_pk_bf16_f32 v84, v4, v5\n\t" \
  "v_cvt_pk_bf16_f32 v85, v6, v7\n\t" \
  "v_mfma_f32_16x16x32_bf16 a[32:35], v[144:147], v[52:55], a[32:35]\n\t" \
  "s_waitcnt lgkmcnt(4)\n\t" \
  "v_exp_f32 v8, v8\n\t" \
  "v_mfma_f32_16x16x32_bf16 a[32:35], v[148:151], v[72:75], a[32:35]\n\t" \
  "ds_read_b64_tr_b16 v[232:233], v170 offset:8192\n\t" \
  "v_mfma_f32_16x16x32_bf16 a[36:39], v[144:147], v[56:59], a[36:39]\n\t" \
  "s_waitcnt lgkmcnt(1)\n\t" \
  "v_exp_f32 v9, v9\n\t" \
  "v_mfma_f32_16x16x32_bf16 a[36:39], v[148:151], v[76:79], a[36:39]\n\t" \
  "buffer_load_dwordx4 v172, %[srd], %[soff] offen offset:3072 lds\n\t" \
  "v_mfma_f32_16x16x32_bf16 v[196:199], v[236:239], v[220:223], v[196:199]\n\t" \
  "ds_read_b64_tr_b16 v[234:235], v170 offset:12288\n\t" \
  "v_cvt_pk_bf16_f32 v88, v8, v9\n\t" \
  "v_mfma_f32_16x16x32_bf16 v[20:23], v[120:123], a[160:163], 0\n\t" \
  "v_exp_f32 v10, v10\n\t" \
  "v_mfma_f32_16x16x32_bf16 v[20:23], v[124:127], a[164:167], v[20:23]\n\t" \
  "ds_read_b64_tr_b16 v[136:137], v158 offset:0\n\t" \
  "v_mov_b32_dpp v244, v218 row_newbcast:5 row_mask:0xf bank_mask:0xf\n\t" \
  "v_mfma_f32_16x16x32_bf16 v[20:23], v[128:131], a[168:171], v[20:23]\n\t" \
  "v_exp_f32 v11, v11\n\t" \
  "v_mfma_f32_16x16x32_bf16 v[20:23], v[132:135], a[172:175], v[20:23]\n\t" \
  "ds_read_b64_tr_b16 v[138:139], v158 offset:4096\n\t" \
  "v_cvt_pk_bf16_f32 v89, v10, v11\n\t" \
  "v_mfma_f32_16x16x32_bf16 v[24:27], v[120:123], a[176:179], 0\n\t" \
  "v_exp_f32 v12, v12\n\t" \
  "v_mfma_f32_16x16x32_bf16 v[24:27], v[124:127], a[180:183], v[24:27]\n\t" \
  "ds_read_b64_tr_b16 v[140:141], v158 offset:8192\n\t" \
  "v_alignbit_b32 v244, v244, v244, v248\n\t" \
  "v_mfma_f32_16x16x32_bf16 v[24:27], v[128:131], a[184:187], v[24:27]\n\t" \
  "v_exp_f32 v13, v13\n\t" \
  "v_mfma_f32_16x16x32_bf16 v[24:27], v[132:135], a[188:191], v[24:27]\n\t" \
  "ds_read_b64_tr_b16 v[142:143], v158 offset:12288\n\t" \
  "v_cvt_pk_bf16_f32 v92, v12, v13\n\t" \
  "v_mfma_f32_16x16x32_bf16 v[28:31], v[120:123], a[192:195], 0\n\t" \
  "v_exp_f32 v14, v14\n\t" \
  "v_mfma_f32_16x16x32_bf16 v[28:31], v[124:127], a[196:199], v[28:31]\n\t" \
  "ds_read_b64_tr_b16 v[144:145], v159 offset:0\n\t" \
  "v_and_b32 v230, v244, v249\n\t" \
  "v_mfma_f32_16x16x32_bf16 v[28:31], v[128:131], a[200:203], v[28:31]\n\t" \
  "v_exp_f32 v15, v15\n\t" \
  "v_mfma_f32_16x16x32_bf16 v[28:31], v[132:135], a[204:207], v[28:31]\n\t" \
  "ds_read_b64_tr_b16 v[146:147], v159 offset:4096\n\t" \
  "v_cvt_pk_bf16_f32 v93, v14, v15\n\t" \
  "v_mfma_f32_16x16x32_bf16 v[32:35], v[120:123], a[208:211], 0\n\t" \
  "v_exp_f32 v16, v16\n\t" \
  "v_mfma_f32_16x16x32_bf16 v[32:35], v[124:127], a[212:215], v[32:35]\n\t" \
  "ds_read_b64_tr_b16 v[148:149], v159 offset:8192\n\t" \
  "v_mov_b32_dpp v245, v219 row_newbcast:5 row_mask:0xf bank_mask:0xf\n\t" \
  "v_mfma_f32_16x16x32_bf16 v[32:35], v[128:131], a[216:219], v[32:35]\n\t" \
  "v_exp_f32 v17, v17\n\t" \
  "v_mfma_f32_16x16x32_bf16 v[32:35], v[132:135], a[220:223], v[32:35]\n\t" \
  "ds_read_b64_tr_b16 v[150:151], v159 offset:12288\n\t" \
  "v_cvt_pk_bf16_f32 v96, v16, v17\n\t" \
  "v_mfma_f32_16x16x32_bf16 v[36:39], v[120:123], a[224:227], 0\n\t" \
  "s_waitcnt lgkmcnt(8)\n\t" \
  "v_exp_f32 v18, v18\n\t" \
  "v_mfma_f32_16x16x32_bf16 v[36:39], v[124:127], a[228:231], v[36:39]\n\t" \
  "ds_read_b64_tr_b16 v[236:237], v171 offset:8192\n\t" \
  "v_alignbit_b32 v245, v245, v245, v248\n\t" \
  "v_mfma_f32_16x16x32_bf16 v[36:39], v[128:131], a[232:235], v[36:39]\n\t" \
  "s_waitcnt lgkmcnt(5)\n\t" \
  "v_exp_f32 v19, v19\n\t" \
  "v_mfma_f32_16x16x32_bf16 v[36:39], v[132:135], a[236:239], v[36:39]\n\t" \
  "v_cvt_pk_bf16_f32 v97, v18, v19\n\t" \
  "v_and_b32 v231, v245, v249\n\t" \
  "v_mfma_f32_16x16x32_bf16 v[200:203], v[232:235], v[220:223], v[200:203]\n\t" \
  "ds_read_b64_tr_b16 v[238:239], v171 offset:12288\n\t" \
  "v_mfma_f32_16x16x32_bf16 a[40:43], v[136:139], v[40:43], a[40:43]\n\t" \
  "v_exp_f32 v20, v20\n\t" \
  "v_mfma_f32_16x16x32_bf16 a[40:43], v[140:143], v[60:63], a[40:43]\n\t" \
  "ds_read_b128 v[120:123], v152 offset:24576\n\t" \
  "v_xor_b32_dpp v240, v208, v247 row_newbcast:5 row_mask:0xf bank_mask:0xf\n\t" \
  "v_mfma_f32_16x16x32_bf16 a[44:47], v[136:139], v[44:47], a[44:47]\n\t" \
  "v_exp_f32 v21, v21\n\t" \
  "v_mfma_f32_16x16x32_bf16 a[44:47], v[140:143], v[64:67], a[44:47]\n\t" \
  "ds_read_b128 v[124:127], v153 offset:24576\n\t" \
  "v_cvt_pk_bf16_f32 v82, v20, v21\n\t" \
  "v_mfma_f32_16x16x32_bf16 a[48:51], v[136:139], v[48:51], a[48:51]\n\t" \
  "s_waitcnt lgkmcnt(4)\n\t" \
  "v_exp_f32 v22, v22\n\t" \
  "v_mfma_f32_16x16x32_bf16 a[48:51], v[140:143], v[68:71], a[48:51]\n\t" \
  "ds_read_b128 v[128:131], v154 offset:24576\n\t" \
  "v_xor_b32_dpp v241, v209, v247 row_newbcast:5 row_mask:0xf bank_mask:0xf\n\t" \
  "v_mfma_f32_16x16x32_bf16 a[52:55], v[136:139], v[52:55], a[52:55]\n\t" \
  "s_add_u32 %[soff], %[soff], 4096\n\t" \
  "v_exp_f32 v23, v23\n\t" \
  "v_mfma_f32_16x16x32_bf16 a[52:55], v[140:143], v[72:75], a[52:55]\n\t" \
  "ds_read_b128 v[132:135], v155 offset:24576\n\t" \
  "v_cvt_pk_bf16_f32 v83, v22, v23\n\t" \
  "v_mfma_f32_16x16x32_bf16 a[56:59], v[136:139], v[56:59], a[56:59]\n\t" \
  "s_add_u32 m0, s72, 36864\n\t" \
  "v_exp_f32 v24, v24\n\t" \
  "v_mfma_f32_16x16x32_bf16 a[56:59], v[140:143], v[76:79], a[56:59]\n\t" \
  "buffer_load_dwordx4 v172, %[srd], %[soff] offen offset:0 lds\n\t" \
  "v_mfma_f32_16x16x32_bf16 a[60:63], v[144:147], v[40:43], a[60:63]\n\t" \
  "v_exp_f32 v25, v25\n\t" \
  "v_mfma_f32_16x16x32_bf16 a[60:63], v[148:151], v[60:63], a[60:63]\n\t" \
  "buffer_load_dwordx4 v172, %[srd], %[soff] offen offset:1024 lds\n\t" \
  "v_mfma_f32_16x16x32_bf16 a[64:67], v[144:147], v[44:47], a[64:67]\n\t" \
  "v_exp_f32 v26, v26\n\t" \
  "v_mfma_f32_16x16x32_bf16 a[64:67], v[148:151], v[64:67], a[64:67]\n\t" \
  "buffer_load_dwordx4 v172, %[srd], %[soff] offen offset:2048 lds\n\t" \
  "v_mfma_f32_16x16x32_bf16 a[68:71], v[144:147], v[48:51], a[68:71]\n\t" \
  "s_waitcnt vmcnt(7)\n\t" \
  "v_exp_f32 v27, v27\n\t" \
  "v_mfma_f32_16x16x32_bf16 a[68:71], v[148:151], v[68:71], a[68:71]\n\t" \
  "v_cvt_pk_bf16_f32 v86, v24, v25\n\t" \
  "v_cvt_pk_bf16_f32 v87, v26, v27\n\t" \
  "v_mfma_f32_16x16x32_bf16 a[72:75], v[144:147], v[52:55], a[72:75]\n\t" \
  "s_waitcnt lgkmcnt(4)\n\t" \
  "v_exp_f32 v28, v28\n\t" \
  "v_mfma_f32_16x16x32_bf16 a[72:75], v[148:151], v[72:75], a[72:75]\n\t" \
  "ds_read_b64_tr_b16 v[232:233], v164 offset:0\n\t" \
  "v_xor_b32_dpp v242, v210, v247 row_newbcast:5 row_mask:0xf bank_mask:0xf\n\t" \
  "v_mfma_f32_16x16x32_bf16 a[76:79], v[144:147], v[56:59], a[76:79]\n\t" \
  "s_waitcnt lgkmcnt(1)\n\t" \
  "v_exp_f32 v29, v29\n\t" \
  "v_mfma_f32_16x16x32_bf16 a[76:79], v[148:151], v[76:79], a[76:79]\n\t" \
  "buffer_load_dwordx4 v172, %[srd], %[soff] offen offset:3072 lds\n\t" \
  "v_mfma_f32_16x16x32_bf16 v[204:207], v[236:239], v[220:223], v[204:207]\n\t" \
  "ds_read_b64_tr_b16 v[234:235], v164 offset:4096\n\t" \
  "v_cvt_pk_bf16_f32 v90, v28, v29\n\t" \
  "v_mfma_f32_16x16x32_bf16 v[0:3], v[120:123], a[160:163], 0\n\t" \
  "v_exp_f32 v30, v30\n\t" \
  "v_mfma_f32_16x16x32_bf16 v[0:3], v[124:127], a[164:167], v[0:3]\n\t" \
  "ds_read_b64_tr_b16 v[136:137], v160 offset:0\n\t" \
  "v_xor_b32_dpp v243, v211, v247 row_newbcast:5 row_mask:0xf bank_mask:0xf\n\t" \
  "v_mfma_f32_16x16x32_bf16 v[0:3], v[128:131], a[168:171], v[0:3]\n\t" \
  "v_exp_f32 v31, v31\n\t" \
  "v_mfma_f32_16x16x32_bf16 v[0:3], v[132:135], a[172:175], v[0:3]\n\t" \
  "ds_read_b64_tr_b16 v[138:139], v160 offset:4096\n\t" \
  "v_cvt_pk_bf16_f32 v91, v30, v31\n\t" \
  "v_mfma_f32_16x16x32_bf16 v[4:7], v[120:123], a[176:179], 0\n\t" \
  "v_exp_f32 v32, v32\n\t" \
  "v_mfma_f32_16x16x32_bf16 v[4:7], v[124:127], a[180:183], v[4:7]\n\t" \
  "ds_read_b64_tr_b16 v[140:141], v160 offset:8192\n\t" \
  "v_mfma_f32_16x16x32_bf16 v[4:7], v[128:131], a[184:187], v[4:7]\n\t" \
  "v_exp_f32 v33, v33\n\t" \
  "v_mfma_f32_16x16x32_bf16 v[4:7], v[132:135], a[188:191], v[4:7]\n\t" \
  "ds_read_b64_tr_b16 v[142:143], v160 offset:12288\n\t" \
  "v_cvt_pk_bf16_f32 v94, v32, v33\n\t" \
  "v_mfma_f32_16x16x32_bf16 v[8:11], v[120:123], a[192:195], 0\n\t" \
  "v_exp_f32 v34, v34\n\t" \
  "v_mfma_f32_16x16x32_bf16 v[8:11], v[124:127], a[196:199], v[8:11]\n\t" \
  "ds_read_b64_tr_b16 v[144:145], v161 offset:0\n\t" \
  "v_mfma_f32_16x16x32_bf16 v[8:11], v[128:131], a[200:203], v[8:11]\n\t" \
  "v_exp_f32 v35, v35\n\t" \
  "v_mfma_f32_16x16x32_bf16 v[8:11], v[132:135], a[204:207], v[8:11]\n\t" \
  "ds_read_b64_tr_b16 v[146:147], v161 offset:4096\n\t" \
  "v_cvt_pk_bf16_f32 v95, v34, v35\n\t" \
  "v_mfma_f32_16x16x32_bf16 v[12:15], v[120:123], a[208:211], 0\n\t" \
  "v_exp_f32 v36, v36\n\t" \
  "v_mfma_f32_16x16x32_bf16 v[12:15], v[124:127], a[212:215], v[12:15]\n\t" \
  "ds_read_b64_tr_b16 v[148:149], v161 offset:8192\n\t" \
  "v_mfma_f32_16x16x32_bf16 v[12:15], v[128:131], a[216:219], v[12:15]\n\t" \
  "v_exp_f32 v37, v37\n\t" \
  "v_mfma_f32_16x16x32_bf16 v[12:15], v[132:135], a[220:223], v[12:15]\n\t" \
  "ds_read_b64_tr_b16 v[150:151], v161 offset:12288\n\t" \
  "v_cvt_pk_bf16_f32 v98, v36, v37\n\t" \
  "v_mfma_f32_16x16x32_bf16 v[16:19], v[120:123], a[224:227], 0\n\t" \
  "s_waitcnt lgkmcnt(8)\n\t" \
  "v_exp_f32 v38, v38\n\t" \
  "v_mfma_f32_16x16x32_bf16 v[16:19], v[124:127], a[228:231], v[16:19]\n\t" \
  "ds_read_b64_tr_b16 v[236:237], v165 offset:0\n\t" \
  "v_mfma_f32_16x16x32_bf16 v[16:19], v[128:131], a[232:235], v[16:19]\n\t" \
  "s_waitcnt lgkmcnt(5)\n\t" \
  "v_exp_f32 v39, v39\n\t" \
  "v_mfma_f32_16x16x32_bf16 v[16:19], v[132:135], a[236:239], v[16:19]\n\t" \
  "v_cvt_pk_bf16_f32 v99, v38, v39\n\t" \
  "v_mfma_f32_16x16x32_bf16 v[176:179], v[232:235], v[224:227], v[176:179]\n\t" \
  "ds_read_b64_tr_b16 v[238:239], v165 offset:4096\n\t" \
  "v_mfma_f32_16x16x32_bf16 a[80:83], v[136:139], v[40:43], a[80:83]\n\t" \
  "v_exp_f32 v0, v0\n\t" \
  "v_mfma_f32_16x16x32_bf16 a[80:83], v[140:143], v[60:63], a[80:83]\n\t" \
  "ds_read_b128 v[120:123], v152 offset:28672\n\t" \
  "v_mfma_f32_16x16x32_bf16 a[84:87], v[136:139], v[44:47], a[84:87]\n\t" \
  "v_exp_f32 v1, v1\n\t" \
  "v_mfma_f32_16x16x32_bf16 a[84:87], v[140:143], v[64:67], a[84:87]\n\t" \
  "ds_read_b128 v[124:127], v153 offset:28672\n\t" \
  "v_cvt_pk_bf16_f32 v100, v0, v1\n\t" \
  "v_mfma_f32_16x16x32_bf16 a[88:91], v[136:139], v[48:51], a[88:91]\n\t" \
  "s_waitcnt lgkmcnt(4)\n\t" \
  "v_exp_f32 v2, v2\n\t" \
  "v_mfma_f32_16x16x32_bf16 a[88:91], v[140:143], v[68:71], a[88:91]\n\t" \
  "ds_read_b128 v[128:131], v154 offset:28672\n\t" \
  "v_mfma_f32_16x16x32_bf16 a[92:95], v[136:139], v[52:55], a[92:95]\n\t" \
  "v_exp_f32 v3, v3\n\t" \
  "v_mfma_f32_16x16x32_bf16 a[92:95], v[140:143], v[72:75], a[92:95]\n\t" \
  "ds_read_b128 v[132:135], v155 offset:28672\n\t" \
  "v_cvt_pk_bf16_f32 v101, v2, v3\n\t" \
  "v_mfma_f32_16x16x32_bf16 a[96:99], v[136:139], v[56:59], a[96:99]\n\t" \
  "s_add_u32 m0, s74, 8192\n\t" \
  "v_exp_f32 v4, v4\n\t" \
  "v_mfma_f32_16x16x32_bf16 a[96:99], v[140:143], v[76:79], a[96:99]\n\t" \
  "buffer_load_dwordx4 v240, %[srdg], s84 offen offset:0 lds\n\t" \
  "v_mfma_f32_16x16x32_bf16 a[100:103], v[144:147], v[40:43], a[100:103]\n\t" \
  "v_exp_f32 v5, v5\n\t" \
  "v_mfma_f32_16x16x32_bf16 a[100:103], v[148:151], v[60:63], a[100:103]\n\t" \
  "buffer_load_dwordx4 v241, %[srdg], s85 offen offset:1024 lds\n\t" \
  "v_mfma_f32_16x16x32_bf16 a[104:107], v[144:147], v[44:47], a[104:107]\n\t" \
  "v_exp_f32 v6, v6\n\t" \
  "v_mfma_f32_16x16x32_bf16 a[104:107], v[148:151], v[64:67], a[104:107]\n\t" \
  "buffer_load_dwordx4 v242, %[srdg], s86 offen offset:2048 lds\n\t" \
  "v_mfma_f32_16x16x32_bf16 a[108:111], v[144:147], v[48:51], a[108:111]\n\t" \
  "v_exp_f32 v7, v7\n\t" \
  "v_mfma_f32_16x16x32_bf16 a[108:111], v[148:151], v[68:71], a[108:111]\n\t" \
  "v_cvt_pk_bf16_f32 v104, v4, v5\n\t" \
  "v_cvt_pk_bf16_f32 v105, v6, v7\n\t" \
  "v_mfma_f32_16x16x32_bf16 a[112:115], v[144:147], v[52:55], a[112:115]\n\t" \
  "s_waitcnt lgkmcnt(4)\n\t" \
  "v_exp_f32 v8, v8\n\t" \
  "v_mfma_f32_16x16x32_bf16 a[112:115], v[148:151], v[72:75], a[112:115]\n\t" \
  "ds_read_b64_tr_b16 v[232:233], v166 offset:0\n\t" \
  "v_xor_b32_dpp v240, v212, v247 row_newbcast:5 row_mask:0xf bank_mask:0xf\n\t" \
  "v_mfma_f32_16x16x32_bf16 a[116:119], v[144:147], v[56:59], a[116:119]\n\t" \
  "s_waitcnt lgkmcnt(1)\n\t" \
  "v_exp_f32 v9, v9\n\t" \
  "v_mfma_f32_16x16x32_bf16 a[116:119], v[148:151], v[76:79], a[116:119]\n\t" \
  "buffer_load_dwordx4 v243, %[srdg], s87 offen offset:3072 lds\n\t" \
  "v_mfma_f32_16x16x32_bf16 v[180:183], v[236:239], v[224:227], v[180:183]\n\t" \
  "ds_read_b64_tr_b16 v[234:235], v166 offset:4096\n\t" \
  "v_cvt_pk_bf16_f32 v108, v8, v9\n\t" \
  "v_mfma_f32_16x16x32_bf16 v[20:23], v[120:123], a[160:163], 0\n\t" \
  "v_exp_f32 v10, v10\n\t" \
  "v_mfma_f32_16x16x32_bf16 v[20:23], v[124:127], a[164:167], v[20:23]\n\t" \
  "ds_read_b64_tr_b16 v[136:137], v162 offset:0\n\t" \
  "v_xor_b32_dpp v241, v213, v247 row_newbcast:5 row_mask:0xf bank_mask:0xf\n\t" \
  "v_mfma_f32_16x16x32_bf16 v[20:23], v[128:131], a[168:171], v[20:23]\n\t" \
  "v_exp_f32 v11, v11\n\t" \
  "v_mfma_f32_16x16x32_bf16 v[20:23], v[132:135], a[172:175], v[20:23]\n\t" \
  "ds_read_b64_tr_b16 v[138:139], v162 offset:4096\n\t" \
  "v_cvt_pk_bf16_f32 v109, v10, v11\n\t" \
  "v_mfma_f32_16x16x32_bf16 v[24:27], v[120:123], a[176:179], 0\n\t" \
  "v_exp_f32 v12, v12\n\t" \
  "v_mfma_f32_16x16x32_bf16 v[24:27], v[124:127], a[180:183], v[24:27]\n\t" \
  "ds_read_b64_tr_b16 v[140:141], v162 offset:8192\n\t" \
  "v_xor_b32_dpp v242, v214, v247 row_newbcast:5 row_mask:0xf bank_mask:0xf\n\t" \
  "v_mfma_f32_16x16x32_bf16 v[24:27], v[128:131], a[184:187], v[24:27]\n\t" \
  "v_exp_f32 v13, v13\n\t" \
  "v_mfma_f32_16x16x32_bf16 v[24:27], v[132:135], a[188:191], v[24:27]\n\t" \
  "ds_read_b64_tr_b16 v[142:143], v162 offset:12288\n\t" \
  "v_cvt_pk_bf16_f32 v112, v12, v13\n\t" \
  "v_mfma_f32_16x16x32_bf16 v[28:31], v[120:123], a[192:195], 0\n\t" \
  "v_exp_f32 v14, v14\n\t" \
  "v_mfma_f32_16x16x32_bf16 v[28:31], v[124:127], a[196:199], v[28:31]\n\t" \
  "ds_read_b64_tr_b16 v[144:145], v163 offset:0\n\t" \
  "v_xor_b32_dpp v243, v215, v247 row_newbcast:5 row_mask:0xf bank_mask:0xf\n\t" \
  "v_mfma_f32_16x16x32_bf16 v[28:31], v[128:131], a[200:203], v[28:31]\n\t" \
  "v_exp_f32 v15, v15\n\t" \
  "v_mfma_f32_16x16x32_bf16 v[28:31], v[132:135], a[204:207], v[28:31]\n\t" \
  "ds_read_b64_tr_b16 v[146:147], v163 offset:4096\n\t" \
  "v_cvt_pk_bf16_f32 v113, v14, v15\n\t" \
  "v_mfma_f32_16x16x32_bf16 v[32:35], v[120:123], a[208:211], 0\n\t" \
  "v_exp_f32 v16, v16\n\t" \
  "v_mfma_f32_16x16x32_bf16 v[32:35], v[124:127], a[212:215], v[32:35]\n\t" \
  "ds_read_b64_tr_b16 v[148:149], v163 offset:8192\n\t" \
  "v_mfma_f32_16x16x32_bf16 v[32:35], v[128:131], a[216:219], v[32:35]\n\t" \
  "v_exp_f32 v17, v17\n\t" \
  "v_mfma_f32_16x16x32_bf16 v[32:35], v[132:135], a[220:223], v[32:35]\n\t" \
  "ds_read_b64_tr_b16 v[150:151], v163 offset:12288\n\t" \
  "v_cvt_pk_bf16_f32 v116, v16, v17\n\t" \
  "v_mfma_f32_16x16x32_bf16 v[36:39], v[120:123], a[224:227], 0\n\t" \
  "s_waitcnt lgkmcnt(8)\n\t" \
  "v_exp_f32 v18, v18\n\t" \
  "v_mfma_f32_16x16x32_bf16 v[36:39], v[124:127], a[228:231], v[36:39]\n\t" \
  "ds_read_b64_tr_b16 v[236:237], v167 offset:0\n\t" \
  "v_mfma_f32_16x16x32_bf16 v[36:39], v[128:131], a[232:235], v[36:39]\n\t" \
  "s_waitcnt lgkmcnt(5)\n\t" \
  "v_exp_f32 v19, v19\n\t" \
  "v_mfma_f32_16x16x32_bf16 v[36:39], v[132:135], a[236:239], v[36:39]\n\t" \
  "v_cvt_pk_bf16_f32 v117, v18, v19\n\t" \
  "v_mfma_f32_16x16x32_bf16 v[184:187], v[232:235], v[224:227], v[184:187]\n\t" \
  "ds_read_b64_tr_b16 v[238:239], v167 offset:4096\n\t" \
  "v_mfma_f32_16x16x32_bf16 a[120:123], v[136:139], v[40:43], a[120:123]\n\t" \
  "s_add_u32 m0, s74, 12288\n\t" \
  "v_exp_f32 v20, v20\n\t" \
  "v_mfma_f32_16x16x32_bf16 a[120:123], v[140:143], v[60:63], a[120:123]\n\t" \
  "buffer_load_dwordx4 v240, %[srdg], s84 offen offset:0 lds\n\t" \
  "v_mfma_f32_16x16x32_bf16 a[124:127], v[136:139], v[44:47], a[124:127]\n\t" \
  "v_exp_f32 v21, v21\n\t" \
  "v_mfma_f32_16x16x32_bf16 a[124:127], v[140:143], v[64:67], a[124:127]\n\t" \
  "buffer_load_dwordx4 v241, %[srdg], s85 offen offset:1024 lds\n\t" \
  "v_mfma_f32_16x16x32_bf16 a[128:131], v[136:139], v[48:51], a[128:131]\n\t" \
  "s_waitcnt lgkmcnt(2)\n\t" \
  "v_exp_f32 v22, v22\n\t" \
  "v_mfma_f32_16x16x32_bf16 a[128:131], v[140:143], v[68:71], a[128:131]\n\t" \
  "buffer_load_dwordx4 v242, %[srdg], s86 offen offset:2048 lds\n\t" \
  "v_mfma_f32_16x16x32_bf16 a[132:135], v[136:139], v[52:55], a[132:135]\n\t" \
  "s_waitcnt vmcnt(7)\n\t" \
  "s_barrier\n\t" \
  "v_exp_f32 v23, v23\n\t" \
  "v_mfma_f32_16x16x32_bf16 a[132:135], v[140:143], v[72:75], a[132:135]\n\t" \
  "ds_read_b128 v[120:123], v152 offset:32768\n\t" \
  "v_cvt_pk_bf16_f32 v102, v20, v21\n\t" \
  "v_mfma_f32_16x16x32_bf16 a[136:139], v[136:139], v[56:59], a[136:139]\n\t" \
  "v_exp_f32 v24, v24\n\t" \
  "v_mfma_f32_16x16x32_bf16 a[136:139], v[140:143], v[76:79], a[136:139]\n\t" \
  "ds_read_b128 v[124:127], v153 offset:32768\n\t" \
  "v_cvt_pk_bf16_f32 v103, v22, v23\n\t" \
  "v_mfma_f32_16x16x32_bf16 a[140:143], v[144:147], v[40:43], a[140:143]\n\t" \
  "v_exp_f32 v25, v25\n\t" \
  "v_mfma_f32_16x16x32_bf16 a[140:143], v[148:151], v[60:63], a[140:143]\n\t" \
  "ds_read_b128 v[128:131], v154 offset:32768\n\t" \
  "v_cvt_pk_bf16_f32 v106, v24, v25\n\t" \
  "v_mfma_f32_16x16x32_bf16 a[144:147], v[144:147], v[44:47], a[144:147]\n\t" \
  "v_exp_f32 v26, v26\n\t" \
  "v_mfma_f32_16x16x32_bf16 a[144:147], v[148:151], v[64:67], a[144:147]\n\t" \
  "ds_read_b128 v[132:135], v155 offset:32768\n\t" \
  "v_mfma_f32_16x16x32_bf16 a[148:151], v[144:147], v[48:51], a[148:151]\n\t" \
  "v_exp_f32 v27, v27\n\t" \
  "v_mfma_f32_16x16x32_bf16 a[148:151], v[148:151], v[68:71], a[148:151]\n\t" \
  "v_cvt_pk_bf16_f32 v107, v26, v27\n\t" \
  "v_mfma_f32_16x16x32_bf16 a[152:155], v[144:147], v[52:55], a[152:155]\n\t" \
  "s_waitcnt lgkmcnt(4)\n\t" \
  "v_exp_f32 v28, v28\n\t" \
  "v_mfma_f32_16x16x32_bf16 a[152:155], v[148:151], v[72:75], a[152:155]\n\t" \
  "ds_read_b64_tr_b16 v[232:233], v168 offset:0\n\t" \
  "v_mfma_f32_16x16x32_bf16 a[156:159], v[144:147], v[56:59], a[156:159]\n\t" \
  "s_waitcnt lgkmcnt(1)\n\t" \
  "v_exp_f32 v29, v29\n\t" \
  "v_mfma_f32_16x16x32_bf16 a[156:159], v[148:151], v[76:79], a[156:159]\n\t" \
  "buffer_load_dwordx4 v243, %[srdg], s87 offen offset:3072 lds\n\t" \
  "v_mfma_f32_16x16x32_bf16 v[188:191], v[236:239], v[224:227], v[188:191]\n\t" \
  "ds_read_b64_tr_b16 v[234:235], v168 offset:4096\n\t" \
  "v_cvt_pk_bf16_f32 v110, v28, v29\n\t" \
  "v_mov_b32_dpp v208, v208 row_shl:6 row_mask:0xf bank_mask:0xf bound_ctrl:0\n\t" \
  "v_mov_b32_dpp v209, v209 row_shl:6 row_mask:0xf bank_mask:0xf bound_ctrl:0\n\t" \
  "v_mov_b32_dpp v210, v210 row_shl:6 row_mask:0xf bank_mask:0xf bound_ctrl:0\n\t" \
  "v_mov_b32_dpp v211, v211 row_shl:6 row_mask:0xf bank_mask:0xf bound_ctrl:0\n\t" \
  "v_mov_b32_dpp v212, v212 row_shl:6 row_mask:0xf bank_mask:0xf bound_ctrl:0\n\t" \
  "v_mov_b32_dpp v213, v213 row_shl:6 row_mask:0xf bank_mask:0xf bound_ctrl:0\n\t" \
  "v_mov_b32_dpp v214, v214 row_shl:6 row_mask:0xf bank_mask:0xf bound_ctrl:0\n\t" \
  "v_mov_b32_dpp v215, v215 row_shl:6 row_mask:0xf bank_mask:0xf bound_ctrl:0\n\t" \
  "v_mov_b32_dpp v216, v216 row_shl:6 row_mask:0xf bank_mask:0xf bound_ctrl:0\n\t" \
  "v_mov_b32_dpp v217, v217 row_shl:6 row_mask:0xf bank_mask:0xf bound_ctrl:0\n\t" \
  "v_mov_b32_dpp v218, v218 row_shl:6 row_mask:0xf bank_mask:0xf bound_ctrl:0\n\t" \
  "v_mov_b32_dpp v219, v219 row_shl:6 row_mask:0xf bank_mask:0xf bound_ctrl:0\n\t" \
  "s_sub_u32 s88, s88, 1\n\t" \
  "s_cmp_lg_u32 s88, 0\n\t" \
  "s_cbranch_scc1 1b\n\t" \
  "s_waitcnt lgkmcnt(0)\n\t" \
  "global_load_ushort v250, %[drow], %[degp]\n\t" \
  "v_mov_b32 v240, v172\n\t" \
  "v_add_u32 v241, 0x1000, v172\n\t" \
  "v_add_u32 v242, 0x8000, v172\n\t" \
  "v_add_u32 v243, 0x9000, v172\n\t" \
  "v_and_b32 v244, 0xff, %[xo]\n\t" \
  "v_add_u32 v244, 2559744, v244\n\t" \
  "v_add_u32 v245, 0, %[xo]\n\t" \
  "v_min_u32 v245, v245, v244\n\t" \
  "global_load_dwordx4 a[224:227], v245, %[xp]\n\t" \
  "v_add_u32 v245, 2560, %[xo]\n\t" \
  "v_min_u32 v245, v245, v244\n\t" \
  "global_load_dwordx4 a[228:231], v245, %[xp]\n\t" \
  "v_add_u32 v245, 5120, %[xo]\n\t" \
  "v_min_u32 v245, v245, v244\n\t" \
  "global_load_dwordx4 a[232:235], v245, %[xp]\n\t" \
  "v_add_u32 v245, 7680, %[xo]\n\t" \
  "v_min_u32 v245, v245, v244\n\t" \
  "global_load_dwordx4 a[236:239], v245, %[xp]\n\t" \
  "s_barrier\n\t" \
  "v_add_u32 v164, s74, v172\n\t" \
  "v_add_u32 v165, s76, v172\n\t" \
  "v_add_u32 v166, s77, v172\n\t" \
  "v_add_u32 v167, s78, v172\n\t" \
  "v_add_u32 v168, s75, v172\n\t" \
  "s_cmp_eq_u32 s80, 0\n\t" \
  "s_cbranch_scc0 2f\n\t" \
  "ds_read_b64_tr_b16 v[136:137], v160 offset:16384\n\t" \
  "ds_read_b64_tr_b16 v[138:139], v160 offset:20480\n\t" \
  "ds_read_b64_tr_b16 v[140:141], v160 offset:24576\n\t" \
  "ds_read_b64_tr_b16 v[142:143], v160 offset:28672\n\t" \
  "ds_read_b64_tr_b16 v[144:145], v161 offset:16384\n\t" \
  "ds_read_b64_tr_b16 v[146:147], v161 offset:20480\n\t" \
  "ds_read_b64_tr_b16 v[148:149], v161 offset:24576\n\t" \
  "ds_read_b64_tr_b16 v[150:151], v161 offset:28672\n\t" \
  "v_exp_f32 v30, v30\n\t" \
  "v_exp_f32 v31, v31\n\t" \
  "v_exp_f32 v32, v32\n\t" \
  "v_exp_f32 v33, v33\n\t" \
  "v_exp_f32 v34, v34\n\t" \
  "v_exp_f32 v35, v35\n\t" \
  "v_exp_f32 v36, v36\n\t" \
  "v_exp_f32 v37, v37\n\t" \
  "v_exp_f32 v38, v38\n\t" \
  "v_exp_f32 v39, v39\n\t" \
  "s_nop 0\n\t" \
  "v_cvt_pk_bf16_f32 v111, v30, v31\n\t" \
  "v_cvt_pk_bf16_f32 v114, v32, v33\n\t" \
  "v_cvt_pk_bf16_f32 v115, v34, v35\n\t" \
  "v_cvt_pk_bf16_f32 v118, v36, v37\n\t" \
  "v_cvt_pk_bf16_f32 v119, v38, v39\n\t" \
  "s_waitcnt lgkmcnt(4)\n\t" \
  "s_nop 1\n\t" \
  "v_mfma_f32_16x16x32_bf16 a[80:83], v[136:139], v[80:83], a[80:83]\n\t" \
  "s_waitcnt vmcnt(4)\n\t" \
  "v_lshlrev_b32 v250, 16, v250\n\t" \
  "v_mul_f32 v250, 0.5, v250\n\t" \
  "v_mfma_f32_16x16x32_bf16 a[80:83], v[140:143], v[100:103], a[80:83]\n\t" \
  "ds_read_b64_tr_b16 v[120:121], v162 offset:16384\n\t" \
  "v_mul_f32 v176, v176, v250\n\t" \
  "v_mfma_f32_16x16x32_bf16 a[84:87], v[136:139], v[84:87], a[84:87]\n\t" \
  "v_mul_f32 v177, v177, v250\n\t" \
  "v_mul_f32 v178, v178, v250\n\t" \
  "v_mfma_f32_16x16x32_bf16 a[84:87], v[140:143], v[104:107], a[84:87]\n\t" \
  "ds_read_b64_tr_b16 v[122:123], v162 offset:20480\n\t" \
  "v_mul_f32 v179, v179, v250\n\t" \
  "v_mfma_f32_16x16x32_bf16 a[88:91], v[136:139], v[88:91], a[88:91]\n\t" \
  "s_waitcnt lgkmcnt(2)\n\t" \
  "v_mul_f32 v180, v180, v250\n\t" \
  "v_mul_f32 v181, v181, v250\n\t" \
  "v_mfma_f32_16x16x32_bf16 a[88:91], v[140:143], v[108:111], a[88:91]\n\t" \
  "ds_read_b64_tr_b16 v[124:125], v162 offset:24576\n\t" \
  "v_mul_f32 v182, v182, v250\n\t" \
  "v_mfma_f32_16x16x32_bf16 a[92:95], v[136:139], v[92:95], a[92:95]\n\t" \
  "v_mul_f32 v183, v183, v250\n\t" \
  "v_mul_f32 v184, v184, v250\n\t" \
  "v_mfma_f32_16x16x32_bf16 a[92:95], v[140:143], v[112:115], a[92:95]\n\t" \
  "ds_read_b64_tr_b16 v[126:127], v162 offset:28672\n\t" \
  "v_mul_f32 v185, v185, v250\n\t" \
  "v_mfma_f32_16x16x32_bf16 a[96:99], v[136:139], v[96:99], a[96:99]\n\t" \
  "v_mul_f32 v186, v186, v250\n\t" \
  "v_mul_f32 v187, v187, v250\n\t" \
  "v_mfma_f32_16x16x32_bf16 a[96:99], v[140:143], v[116:119], a[96:99]\n\t" \
  "ds_read_b64_tr_b16 v[128:129], v163 offset:16384\n\t" \
  "v_mul_f32 v188, v188, v250\n\t" \
  "v_mfma_f32_16x16x32_bf16 a[100:103], v[144:147], v[80:83], a[100:103]\n\t" \
  "v_mul_f32 v189, v189, v250\n\t" \
  "v_mul_f32 v190, v190, v250\n\t" \
  "v_mfma_f32_16x16x32_bf16 a[100:103], v[148:151], v[100:103], a[100:103]\n\t" \
  "ds_read_b64_tr_b16 v[130:131], v163 offset:20480\n\t" \
  "v_mul_f32 v191, v191, v250\n\t" \
  "v_mfma_f32_16x16x32_bf16 a[104:107], v[144:147], v[84:87], a[104:107]\n\t" \
  "v_mul_f32 v192, v192, v250\n\t" \
  "v_mul_f32 v193, v193, v250\n\t" \
  "v_mfma_f32_16x16x32_bf16 a[104:107], v[148:151], v[104:107], a[104:107]\n\t" \
  "ds_read_b64_tr_b16 v[132:133], v163 offset:24576\n\t" \
  "v_mul_f32 v194, v194, v250\n\t" \
  "v_mfma_f32_16x16x32_bf16 a[108:111], v[144:147], v[88:91], a[108:111]\n\t" \
  "v_mul_f32 v195, v195, v250\n\t" \
  "v_mul_f32 v196, v196, v250\n\t" \
  "v_mfma_f32_16x16x32_bf16 a[108:111], v[148:151], v[108:111], a[108:111]\n\t" \
  "ds_read_b64_tr_b16 v[134:135], v163 offset:28672\n\t" \
  "v_mul_f32 v197, v197, v250\n\t" \
  "v_mfma_f32_16x16x32_bf16 a[112:115], v[144:147], v[92:95], a[112:115]\n\t" \
  "v_mul_f32 v198, v198, v250\n\t" \
  "v_mul_f32 v199, v199, v250\n\t" \
  "v_mfma_f32_16x16x32_bf16 a[112:115], v[148:151], v[112:115], a[112:115]\n\t" \
  "global_load_dwordx4 a[160:163], v240, %[wfp] offset:0\n\t" \
  "v_mfma_f32_16x16x32_bf16 a[116:119], v[144:147], v[96:99], a[116:119]\n\t" \
  "s_waitcnt lgkmcnt(4)\n\t" \
  "v_mul_f32 v200, v200, v250\n\t" \
  "v_mul_f32 v201, v201, v250\n\t" \
  "v_mfma_f32_16x16x32_bf16 a[116:119], v[148:151], v[116:119], a[116:119]\n\t" \
  "global_load_dwordx4 a[164:167], v240, %[wfp] offset:1024\n\t" \
  "v_mfma_f32_16x16x32_bf16 a[120:123], v[120:123], v[80:83], a[120:123]\n\t" \
  "v_mul_f32 v202, v202, v250\n\t" \
  "v_mul_f32 v203, v203, v250\n\t" \
  "v_mfma_f32_16x16x32_bf16 a[120:123], v[124:127], v[100:103], a[120:123]\n\t" \
  "ds_read_b64_tr_b16 v[136:137], v156 offset:16384\n\t" \
  "v_mul_f32 v204, v204, v250\n\t" \
  "v_mfma_f32_16x16x32_bf16 a[124:127], v[120:123], v[84:87], a[124:127]\n\t" \
  "v_mul_f32 v205, v205, v250\n\t" \
  "v_mul_f32 v206, v206, v250\n\t" \
  "v_mfma_f32_16x16x32_bf16 a[124:127], v[124:127], v[104:107], a[124:127]\n\t" \
  "ds_read_b64_tr_b16 v[138:139], v156 offset:20480\n\t" \
  "v_mul_f32 v207, v207, v250\n\t" \
  "v_mfma_f32_16x16x32_bf16 a[128:131], v[120:123], v[88:91], a[128:131]\n\t" \
  "s_waitcnt lgkmcnt(2)\n\t" \
  "v_cvt_pk_bf16_f32 v216, v176, v177\n\t" \
  "v_cvt_pk_bf16_f32 v217, v178, v179\n\t" \
  "v_mfma_f32_16x16x32_bf16 a[128:131], v[124:127], v[108:111], a[128:131]\n\t" \
  "ds_read_b64_tr_b16 v[140:141], v156 offset:24576\n\t" \
  "v_cvt_pk_bf16_f32 v218, v180, v181\n\t" \
  "v_mfma_f32_16x16x32_bf16 a[132:135], v[120:123], v[92:95], a[132:135]\n\t" \
  "v_cvt_pk_bf16_f32 v219, v182, v183\n\t" \
  "v_cvt_pk_bf16_f32 v220, v184, v185\n\t" \
  "v_mfma_f32_16x16x32_bf16 a[132:135], v[124:127], v[112:115], a[132:135]\n\t" \
  "ds_read_b64_tr_b16 v[142:143], v156 offset:28672\n\t" \
  "v_cvt_pk_bf16_f32 v221, v186, v187\n\t" \
  "v_mfma_f32_16x16x32_bf16 a[136:139], v[120:123], v[96:99], a[136:139]\n\t" \
  "v_cvt_pk_bf16_f32 v222, v188, v189\n\t" \
  "v_cvt_pk_bf16_f32 v223, v190, v191\n\t" \
  "v_mfma_f32_16x16x32_bf16 a[136:139], v[124:127], v[116:119], a[136:139]\n\t" \
  "ds_read_b64_tr_b16 v[144:145], v157 offset:16384\n\t" \
  "v_cvt_pk_bf16_f32 v224, v192, v193\n\t" \
  "v_mfma_f32_16x16x32_bf16 a[140:143], v[128:131], v[80:83], a[140:143]\n\t" \
  "v_cvt_pk_bf16_f32 v225, v194, v195\n\t" \
  "v_cvt_pk_bf16_f32 v226, v196, v197\n\t" \
  "v_mfma_f32_16x16x32_bf16 a[140:143], v[132:135], v[100:103], a[140:143]\n\t" \
  "ds_read_b64_tr_b16 v[146:147], v157 offset:20480\n\t" \
  "v_cvt_pk_bf16_f32 v227, v198, v199\n\t" \
  "v_mfma_f32_16x16x32_bf16 a[144:147], v[128:131], v[84:87], a[144:147]\n\t" \
  "v_cvt_pk_bf16_f32 v228, v200, v201\n\t" \
  "v_cvt_pk_bf16_f32 v229, v202, v203\n\t" \
  "v_mfma_f32_16x16x32_bf16 a[144:147], v[132:135], v[104:107], a[144:147]\n\t" \
  "ds_read_b64_tr_b16 v[148:149], v157 offset:24576\n\t" \
  "v_cvt_pk_bf16_f32 v230, v204, v205\n\t" \
  "v_mfma_f32_16x16x32_bf16 a[148:151], v[128:131], v[88:91], a[148:151]\n\t" \
  "v_cvt_pk_bf16_f32 v231, v206, v207\n\t" \
  "v_mfma_f32_16x16x32_bf16 a[148:151], v[132:135], v[108:111], a[148:151]\n\t" \
  "ds_read_b64_tr_b16 v[150:151], v157 offset:28672\n\t" \
  "v_mfma_f32_16x16x32_bf16 a[152:155], v[128:131], v[92:95], a[152:155]\n\t" \
  "v_mfma_f32_16x16x32_bf16 a[152:155], v[132:135], v[112:115], a[152:155]\n\t" \
  "global_load_dwordx4 a[168:171], v240, %[wfp] offset:2048\n\t" \
  "v_mfma_f32_16x16x32_bf16 a[156:159], v[128:131], v[96:99], a[156:159]\n\t" \
  "s_waitcnt lgkmcnt(4)\n\t" \
  "v_mfma_f32_16x16x32_bf16 a[156:159], v[132:135], v[116:119], a[156:159]\n\t" \
  "global_load_dwordx4 a[172:175], v240, %[wfp] offset:3072\n\t" \
  "v_mfma_f32_16x16x32_bf16 a[0:3], v[136:139], v[80:83], a[0:3]\n\t" \
  "ds_write_b128 v164, a[80:83] offset:0\n\t" \
  "v_mfma_f32_16x16x32_bf16 a[0:3], v[140:143], v[100:103], a[0:3]\n\t" \
  "ds_read_b64_tr_b16 v[120:121], v158 offset:16384\n\t" \
  "v_mfma_f32_16x16x32_bf16 a[4:7], v[136:139], v[84:87], a[4:7]\n\t" \
  "ds_write_b128 v164, a[84:87] offset:1024\n\t" \
  "v_mfma_f32_16x16x32_bf16 a[4:7], v[140:143], v[104:107], a[4:7]\n\t" \
  "ds_read_b64_tr_b16 v[122:123], v158 offset:20480\n\t" \
  "v_mfma_f32_16x16x32_bf16 a[8:11], v[136:139], v[88:91], a[8:11]\n\t" \
  "s_waitcnt lgkmcnt(4)\n\t" \
  "ds_write_b128 v164, a[88:91] offset:2048\n\t" \
  "v_mfma_f32_16x16x32_bf16 a[8:11], v[140:143], v[108:111], a[8:11]\n\t" \
  "ds_read_b64_tr_b16 v[124:125], v158 offset:24576\n\t" \
  "v_mfma_f32_16x16x32_bf16 a[12:15], v[136:139], v[92:95], a[12:15]\n\t" \
  "ds_write_b128 v164, a[92:95] offset:3072\n\t" \
  "v_mfma_f32_16x16x32_bf16 a[12:15], v[140:143], v[112:115], a[12:15]\n\t" \
  "ds_read_b64_tr_b16 v[126:127], v158 offset:28672\n\t" \
  "v_mfma_f32_16x16x32_bf16 a[16:19], v[136:139], v[96:99], a[16:19]\n\t" \
  "ds_write_b128 v164, a[96:99] offset:4096\n\t" \
  "v_mfma_f32_16x16x32_bf16 a[16:19], v[140:143], v[116:119], a[16:19]\n\t" \
  "ds_read_b64_tr_b16 v[128:129], v159 offset:16384\n\t" \
  "v_mfma_f32_16x16x32_bf16 a[20:23], v[144:147], v[80:83], a[20:23]\n\t" \
  "ds_write_b128 v164, a[100:103] offset:5120\n\t" \
  "v_mfma_f32_16x16x32_bf16 a[20:23], v[148:151], v[100:103], a[20:23]\n\t" \
  "ds_read_b64_tr_b16 v[130:131], v159 offset:20480\n\t" \
  "v_mfma_f32_16x16x32_bf16 a[24:27], v[144:147], v[84:87], a[24:27]\n\t" \
  "ds_write_b128 v164, a[104:107] offset:6144\n\t" \
  "v_mfma_f32_16x16x32_bf16 a[24:27], v[148:151], v[104:107], a[24:27]\n\t" \
  "ds_read_b64_tr_b16 v[132:133], v159 offset:24576\n\t" \
  "v_mfma_f32_16x16x32_bf16 a[28:31], v[144:147], v[88:91], a[28:31]\n\t" \
  "ds_write_b128 v164, a[108:111] offset:7168\n\t" \
  "v_mfma_f32_16x16x32_bf16 a[28:31], v[148:151], v[108:111], a[28:31]\n\t" \
  "ds_read_b64_tr_b16 v[134:135], v159 offset:28672\n\t" \
  "v_mfma_f32_16x16x32_bf16 a[32:35], v[144:147], v[92:95], a[32:35]\n\t" \
  "ds_write_b128 v164, a[112:115] offset:8192\n\t" \
  "v_mfma_f32_16x16x32_bf16 a[32:35], v[148:151], v[112:115], a[32:35]\n\t" \
  "global_load_dwordx4 a[176:179], v241, %[wfp] offset:0\n\t" \
  "v_mfma_f32_16x16x32_bf16 a[36:39], v[144:147], v[96:99], a[36:39]\n\t" \
  "s_waitcnt lgkmcnt(9)\n\t" \
  "ds_write_b128 v164, a[116:119] offset:9216\n\t" \
  "v_mfma_f32_16x16x32_bf16 a[36:39], v[148:151], v[116:119], a[36:39]\n\t" \
  "global_load_dwordx4 a[180:183], v241, %[wfp] offset:1024\n\t" \
  "v_mfma_f32_16x16x32_bf16 a[40:43], v[120:123], v[80:83], a[40:43]\n\t" \
  "ds_write_b128 v164, a[120:123] offset:10240\n\t" \
  "v_mfma_f32_16x16x32_bf16 a[40:43], v[124:127], v[100:103], a[40:43]\n\t" \
  "global_load_dwordx4 a[184:187], v241, %[wfp] offset:2048\n\t" \
  "v_mfma_f32_16x16x32_bf16 a[44:47], v[120:123], v[84:87], a[44:47]\n\t" \
  "ds_write_b128 v164, a[124:127] offset:11264\n\t" \
  "v_mfma_f32_16x16x32_bf16 a[44:47], v[124:127], v[104:107], a[44:47]\n\t" \
  "global_load_dwordx4 a[188:191], v241, %[wfp] offset:3072\n\t" \
  "v_mfma_f32_16x16x32_bf16 a[48:51], v[120:123], v[88:91], a[48:51]\n\t" \
  "s_waitcnt lgkmcnt(4)\n\t" \
  "ds_write_b128 v164, a[128:131] offset:12288\n\t" \
  "v_mfma_f32_16x16x32_bf16 a[48:51], v[124:127], v[108:111], a[48:51]\n\t" \
  "global_load_dwordx4 a[192:195], v242, %[wfp] offset:0\n\t" \
  "v_mfma_f32_16x16x32_bf16 a[52:55], v[120:123], v[92:95], a[52:55]\n\t" \
  "ds_write_b128 v164, a[132:135] offset:13312\n\t" \
  "v_mfma_f32_16x16x32_bf16 a[52:55], v[124:127], v[112:115], a[52:55]\n\t" \
  "global_load_dwordx4 a[196:199], v242, %[wfp] offset:1024\n\t" \
  "v_mfma_f32_16x16x32_bf16 a[56:59], v[120:123], v[96:99], a[56:59]\n\t" \
  "ds_write_b128 v164, a[136:139] offset:14336\n\t" \
  "v_mfma_f32_16x16x32_bf16 a[56:59], v[124:127], v[116:119], a[56:59]\n\t" \
  "global_load_dwordx4 a[200:203], v242, %[wfp] offset:2048\n\t" \
  "v_mfma_f32_16x16x32_bf16 a[60:63], v[128:131], v[80:83], a[60:63]\n\t" \
  "ds_write_b128 v164, a[140:143] offset:15360\n\t" \
  "v_mfma_f32_16x16x32_bf16 a[60:63], v[132:135], v[100:103], a[60:63]\n\t" \
  "global_load_dwordx4 a[204:207], v242, %[wfp] offset:3072\n\t" \
  "v_mfma_f32_16x16x32_bf16 a[64:67], v[128:131], v[84:87], a[64:67]\n\t" \
  "ds_write_b128 v165, a[144:147] offset:0\n\t" \
  "v_mfma_f32_16x16x32_bf16 a[64:67], v[132:135], v[104:107], a[64:67]\n\t" \
  "global_load_dwordx4 a[208:211], v243, %[wfp] offset:0\n\t" \
  "v_mfma_f32_16x16x32_bf16 a[68:71], v[128:131], v[88:91], a[68:71]\n\t" \
  "ds_write_b128 v165, a[148:151] offset:1024\n\t" \
  "v_mfma_f32_16x16x32_bf16 a[68:71], v[132:135], v[108:111], a[68:71]\n\t" \
  "global_load_dwordx4 a[212:215], v243, %[wfp] offset:1024\n\t" \
  "v_mfma_f32_16x16x32_bf16 a[72:75], v[128:131], v[92:95], a[72:75]\n\t" \
  "ds_write_b128 v165, a[152:155] offset:2048\n\t" \
  "v_mfma_f32_16x16x32_bf16 a[72:75], v[132:135], v[112:115], a[72:75]\n\t" \
  "global_load_dwordx4 a[216:219], v243, %[wfp] offset:2048\n\t" \
  "v_mfma_f32_16x16x32_bf16 a[76:79], v[128:131], v[96:99], a[76:79]\n\t" \
  "ds_write_b128 v165, a[156:159] offset:3072\n\t" \
  "v_mfma_f32_16x16x32_bf16 a[76:79], v[132:135], v[116:119], a[76:79]\n\t" \
  "global_load_dwordx4 a[220:223], v243, %[wfp] offset:3072\n\t" \
  "ds_write_b128 v168, v[216:219] offset:0\n\t" \
  "ds_write_b128 v168, v[220:223] offset:1024\n\t" \
  "ds_write_b128 v168, v[224:227] offset:2048\n\t" \
  "ds_write_b128 v168, v[228:231] offset:3072\n\t" \
  "s_waitcnt lgkmcnt(0)\n\t" \
  "s_barrier\n\t" \
  "ds_read_b128 v[0:3], v166 offset:0\n\t" \
  "ds_read_b128 v[4:7], v166 offset:1024\n\t" \
  "ds_read_b128 v[8:11], v166 offset:2048\n\t" \
  "ds_read_b128 v[12:15], v166 offset:3072\n\t" \
  "ds_read_b128 v[16:19], v166 offset:4096\n\t" \
  "ds_read_b128 v[20:23], v166 offset:5120\n\t" \
  "ds_read_b128 v[24:27], v166 offset:6144\n\t" \
  "ds_read_b128 v[28:31], v166 offset:7168\n\t" \
  "ds_read_b128 v[32:35], v166 offset:8192\n\t" \
  "ds_read_b128 v[36:39], v166 offset:9216\n\t" \
  "ds_read_b128 v[40:43], v166 offset:10240\n\t" \
  "ds_read_b128 v[44:47], v166 offset:11264\n\t" \
  "ds_read_b128 v[48:51], v166 offset:12288\n\t" \
  "ds_read_b128 v[52:55], v166 offset:13312\n\t" \
  "ds_read_b128 v[56:59], v166 offset:14336\n\t" \
  "ds_read_b128 v[60:63], v166 offset:15360\n\t" \
  "ds_read_b128 v[64:67], v167 offset:0\n\t" \
  "ds_read_b128 v[68:71], v167 offset:1024\n\t" \
  "ds_read_b128 v[72:75], v167 offset:2048\n\t" \
  "ds_read_b128 v[76:79], v167 offset:3072\n\t" \
  "v_accvgpr_read_b32 v80, a0\n\t" \
  "v_accvgpr_read_b32 v81, a1\n\t" \
  "v_accvgpr_read_b32 v82, a2\n\t" \
  "v_accvgpr_read_b32 v83, a3\n\t" \
  "v_accvgpr_read_b32 v84, a4\n\t" \
  "v_accvgpr_read_b32 v85, a5\n\t" \
  "v_accvgpr_read_b32 v86, a6\n\t" \
  "v_accvgpr_read_b32 v87, a7\n\t" \
  "v_accvgpr_read_b32 v88, a8\n\t" \
  "v_accvgpr_read_b32 v89, a9\n\t" \
  "v_accvgpr_read_b32 v90, a10\n\t" \
  "v_accvgpr_read_b32 v91, a11\n\t" \
  "v_accvgpr_read_b32 v92, a12\n\t" \
  "v_accvgpr_read_b32 v93, a13\n\t" \
  "v_accvgpr_read_b32 v94, a14\n\t" \
  "v_accvgpr_read_b32 v95, a15\n\t" \
  "v_accvgpr_read_b32 v96, a16\n\t" \
  "v_accvgpr_read_b32 v97, a17\n\t" \
  "v_accvgpr_read_b32 v98, a18\n\t" \
  "v_accvgpr_read_b32 v99, a19\n\t" \
  "v_accvgpr_read_b32 v100, a20\n\t" \
  "v_accvgpr_read_b32 v101, a21\n\t" \
  "v_accvgpr_read_b32 v102, a22\n\t" \
  "v_accvgpr_read_b32 v103, a23\n\t" \
  "v_accvgpr_read_b32 v104, a24\n\t" \
  "v_accvgpr_read_b32 v105, a25\n\t" \
  "v_accvgpr_read_b32 v106, a26\n\t" \
  "v_accvgpr_read_b32 v107, a27\n\t" \
  "v_accvgpr_read_b32 v108, a28\n\t" \
  "v_accvgpr_read_b32 v109, a29\n\t" \
  "v_accvgpr_read_b32 v110, a30\n\t" \
  "v_accvgpr_read_b32 v111, a31\n\t" \
  "v_accvgpr_read_b32 v112, a32\n\t" \
  "v_accvgpr_read_b32 v113, a33\n\t" \
  "v_accvgpr_read_b32 v114, a34\n\t" \
  "v_accvgpr_read_b32 v115, a35\n\t" \
  "v_accvgpr_read_b32 v116, a36\n\t" \
  "v_accvgpr_read_b32 v117, a37\n\t" \
  "v_accvgpr_read_b32 v118, a38\n\t" \
  "v_accvgpr_read_b32 v119, a39\n\t" \
  "v_accvgpr_read_b32 v120, a40\n\t" \
  "v_accvgpr_read_b32 v121, a41\n\t" \
  "v_accvgpr_read_b32 v122, a42\n\t" \
  "v_accvgpr_read_b32 v123, a43\n\t" \
  "v_accvgpr_read_b32 v124, a44\n\t" \
  "v_accvgpr_read_b32 v125, a45\n\t" \
  "v_accvgpr_read_b32 v126, a46\n\t" \
  "v_accvgpr_read_b32 v127, a47\n\t" \
  "v_accvgpr_read_b32 v128, a48\n\t" \
  "v_accvgpr_read_b32 v129, a49\n\t" \
  "v_accvgpr_read_b32 v130, a50\n\t" \
  "v_accvgpr_read_b32 v131, a51\n\t" \
  "v_accvgpr_read_b32 v132, a52\n\t" \
  "v_accvgpr_read_b32 v133, a53\n\t" \
  "v_accvgpr_read_b32 v134, a54\n\t" \
  "v_accvgpr_read_b32 v135, a55\n\t" \
  "v_accvgpr_read_b32 v136, a56\n\t" \
  "v_accvgpr_read_b32 v137, a57\n\t" \
  "v_accvgpr_read_b32 v138, a58\n\t" \
  "v_accvgpr_read_b32 v139, a59\n\t" \
  "v_accvgpr_read_b32 v140, a60\n\t" \
  "v_accvgpr_read_b32 v141, a61\n\t" \
  "v_accvgpr_read_b32 v142, a62\n\t" \
  "v_accvgpr_read_b32 v143, a63\n\t" \
  "v_accvgpr_read_b32 v144, a64\n\t" \
  "v_accvgpr_read_b32 v145, a65\n\t" \
  "v_accvgpr_read_b32 v146, a66\n\t" \
  "v_accvgpr_read_b32 v147, a67\n\t" \
  "v_accvgpr_read_b32 v148, a68\n\t" \
  "v_accvgpr_read_b32 v149, a69\n\t" \
  "v_accvgpr_read_b32 v150, a70\n\t" \
  "v_accvgpr_read_b32 v151, a71\n\t" \
  "v_accvgpr_read_b32 v152, a72\n\t" \
  "v_accvgpr_read_b32 v153, a73\n\t" \
  "v_accvgpr_read_b32 v154, a74\n\t" \
  "v_accvgpr_read_b32 v155, a75\n\t" \
  "v_accvgpr_read_b32 v156, a76\n\t" \
  "v_accvgpr_read_b32 v157, a77\n\t" \
  "v_accvgpr_read_b32 v158, a78\n\t" \
  "v_accvgpr_read_b32 v159, a79\n\t" \
  "s_branch 3f\n\t" \
  "2:\n\t" \
  "ds_read_b64_tr_b16 v[136:137], v156 offset:16384\n\t" \
  "ds_read_b64_tr_b16 v[138:139], v156 offset:20480\n\t" \
  "ds_read_b64_tr_b16 v[140:141], v156 offset:24576\n\t" \
  "ds_read_b64_tr_b16 v[142:143], v156 offset:28672\n\t" \
  "ds_read_b64_tr_b16 v[144:145], v157 offset:16384\n\t" \
  "ds_read_b64_tr_b16 v[146:147], v157 offset:20480\n\t" \
  "ds_read_b64_tr_b16 v[148:149], v157 offset:24576\n\t" \
  "ds_read_b64_tr_b16 v[150:151], v157 offset:28672\n\t" \
  "v_exp_f32 v30, v30\n\t" \
  "v_exp_f32 v31, v31\n\t" \
  "v_exp_f32 v32, v32\n\t" \
  "v_exp_f32 v33, v33\n\t" \
  "v_exp_f32 v34, v34\n\t" \
  "v_exp_f32 v35, v35\n\t" \
  "v_exp_f32 v36, v36\n\t" \
  "v_exp_f32 v37, v37\n\t" \
  "v_exp_f32 v38, v38\n\t" \
  "v_exp_f32 v39, v39\n\t" \
  "s_nop 0\n\t" \
  "v_cvt_pk_bf16_f32 v111, v30, v31\n\t" \
  "v_cvt_pk_bf16_f32 v114, v32, v33\n\t" \
  "v_cvt_pk_bf16_f32 v115, v34, v35\n\t" \
  "v_cvt_pk_bf16_f32 v118, v36, v37\n\t" \
  "v_cvt_pk_bf16_f32 v119, v38, v39\n\t" \
  "s_waitcnt lgkmcnt(4)\n\t" \
  "s_nop 1\n\t" \
  "v_mfma_f32_16x16x32_bf16 a[0:3], v[136:139], v[80:83], a[0:3]\n\t" \
  "s_waitcnt vmcnt(4)\n\t" \
  "v_lshlrev_b32 v250, 16, v250\n\t" \
  "v_mul_f32 v250, 0.5, v250\n\t" \
  "v_mfma_f32_16x16x32_bf16 a[0:3], v[140:143], v[100:103], a[0:3]\n\t" \
  "ds_read_b64_tr_b16 v[120:121], v158 offset:16384\n\t" \
  "v_mul_f32 v176, v176, v250\n\t" \
  "v_mfma_f32_16x16x32_bf16 a[4:7], v[136:139], v[84:87], a[4:7]\n\t" \
  "v_mul_f32 v177, v177, v250\n\t" \
  "v_mul_f32 v178, v178, v250\n\t" \
  "v_mfma_f32_16x16x32_bf16 a[4:7], v[140:143], v[104:107], a[4:7]\n\t" \
  "ds_read_b64_tr_b16 v[122:123], v158 offset:20480\n\t" \
  "v_mul_f32 v179, v179, v250\n\t" \
  "v_mfma_f32_16x16x32_bf16 a[8:11], v[136:139], v[88:91], a[8:11]\n\t" \
  "s_waitcnt lgkmcnt(2)\n\t" \
  "v_mul_f32 v180, v180, v250\n\t" \
  "v_mul_f32 v181, v181, v250\n\t" \
  "v_mfma_f32_16x16x32_bf16 a[8:11], v[140:143], v[108:111], a[8:11]\n\t" \
  "ds_read_b64_tr_b16 v[124:125], v158 offset:24576\n\t" \
  "v_mul_f32 v182, v182, v250\n\t" \
  "v_mfma_f32_16x16x32_bf16 a[12:15], v[136:139], v[92:95], a[12:15]\n\t" \
  "v_mul_f32 v183, v183, v250\n\t" \
  "v_mul_f32 v184, v184, v250\n\t" \
  "v_mfma_f32_16x16x32_bf16 a[12:15], v[140:143], v[112:115], a[12:15]\n\t" \
  "ds_read_b64_tr_b16 v[126:127], v158 offset:28672\n\t" \
  "v_mul_f32 v185, v185, v250\n\t" \
  "v_mfma_f32_16x16x32_bf16 a[16:19], v[136:139], v[96:99], a[16:19]\n\t" \
  "v_mul_f32 v186, v186, v250\n\t" \
  "v_mul_f32 v187, v187, v250\n\t" \
  "v_mfma_f32_16x16x32_bf16 a[16:19], v[140:143], v[116:119], a[16:19]\n\t" \
  "ds_read_b64_tr_b16 v[128:129], v159 offset:16384\n\t" \
  "v_mul_f32 v188, v188, v250\n\t" \
  "v_mfma_f32_16x16x32_bf16 a[20:23], v[144:147], v[80:83], a[20:23]\n\t" \
  "v_mul_f32 v189, v189, v250\n\t" \
  "v_mul_f32 v190, v190, v250\n\t" \
  "v_mfma_f32_16x16x32_bf16 a[20:23], v[148:151], v[100:103], a[20:23]\n\t" \
  "ds_read_b64_tr_b16 v[130:131], v159 offset:20480\n\t" \
  "v_mul_f32 v191, v191, v250\n\t" \
  "v_mfma_f32_16x16x32_bf16 a[24:27], v[144:147], v[84:87], a[24:27]\n\t" \
  "v_mul_f32 v192, v192, v250\n\t" \
  "v_mul_f32 v193, v193, v250\n\t" \
  "v_mfma_f32_16x16x32_bf16 a[24:27], v[148:151], v[104:107], a[24:27]\n\t" \
  "ds_read_b64_tr_b16 v[132:133], v159 offset:24576\n\t" \
  "v_mul_f32 v194, v194, v250\n\t" \
  "v_mfma_f32_16x16x32_bf16 a[28:31], v[144:147], v[88:91], a[28:31]\n\t" \
  "v_mul_f32 v195, v195, v250\n\t" \
  "v_mul_f32 v196, v196, v250\n\t" \
  "v_mfma_f32_16x16x32_bf16 a[28:31], v[148:151], v[108:111], a[28:31]\n\t" \
  "ds_read_b64_tr_b16 v[134:135], v159 offset:28672\n\t" \
  "v_mul_f32 v197, v197, v250\n\t" \
  "v_mfma_f32_16x16x32_bf16 a[32:35], v[144:147], v[92:95], a[32:35]\n\t" \
  "v_mul_f32 v198, v198, v250\n\t" \
  "v_mul_f32 v199, v199, v250\n\t" \
  "v_mfma_f32_16x16x32_bf16 a[32:35], v[148:151], v[112:115], a[32:35]\n\t" \
  "global_load_dwordx4 a[160:163], v240, %[wfp] offset:0\n\t" \
  "v_mfma_f32_16x16x32_bf16 a[36:39], v[144:147], v[96:99], a[36:39]\n\t" \
  "s_waitcnt lgkmcnt(4)\n\t" \
  "v_mul_f32 v200, v200, v250\n\t" \
  "v_mul_f32 v201, v201, v250\n\t" \
  "v_mfma_f32_16x16x32_bf16 a[36:39], v[148:151], v[116:119], a[36:39]\n\t" \
  "global_load_dwordx4 a[164:167], v240, %[wfp] offset:1024\n\t" \
  "v_mfma_f32_16x16x32_bf16 a[40:43], v[120:123], v[80:83], a[40:43]\n\t" \
  "v_mul_f32 v202, v202, v250\n\t" \
  "v_mul_f32 v203, v203, v250\n\t" \
  "v_mfma_f32_16x16x32_bf16 a[40:43], v[124:127], v[100:103], a[40:43]\n\t" \
  "ds_read_b64_tr_b16 v[136:137], v160 offset:16384\n\t" \
  "v_mul_f32 v204, v204, v250\n\t" \
  "v_mfma_f32_16x16x32_bf16 a[44:47], v[120:123], v[84:87], a[44:47]\n\t" \
  "v_mul_f32 v205, v205, v250\n\t" \
  "v_mul_f32 v206, v206, v250\n\t" \
  "v_mfma_f32_16x16x32_bf16 a[44:47], v[124:127], v[104:107], a[44:47]\n\t" \
  "ds_read_b64_tr_b16 v[138:139], v160 offset:20480\n\t" \
  "v_mul_f32 v207, v207, v250\n\t" \
  "v_mfma_f32_16x16x32_bf16 a[48:51], v[120:123], v[88:91], a[48:51]\n\t" \
  "s_waitcnt lgkmcnt(2)\n\t" \
  "v_cvt_pk_bf16_f32 v216, v176, v177\n\t" \
  "v_cvt_pk_bf16_f32 v217, v178, v179\n\t" \
  "v_mfma_f32_16x16x32_bf16 a[48:51], v[124:127], v[108:111], a[48:51]\n\t" \
  "ds_read_b64_tr_b16 v[140:141], v160 offset:24576\n\t" \
  "v_cvt_pk_bf16_f32 v218, v180, v181\n\t" \
  "v_mfma_f32_16x16x32_bf16 a[52:55], v[120:123], v[92:95], a[52:55]\n\t" \
  "v_cvt_pk_bf16_f32 v219, v182, v183\n\t" \
  "v_cvt_pk_bf16_f32 v220, v184, v185\n\t" \
  "v_mfma_f32_16x16x32_bf16 a[52:55], v[124:127], v[112:115], a[52:55]\n\t" \
  "ds_read_b64_tr_b16 v[142:143], v160 offset:28672\n\t" \
  "v_cvt_pk_bf16_f32 v221, v186, v187\n\t" \
  "v_mfma_f32_16x16x32_bf16 a[56:59], v[120:123], v[96:99], a[56:59]\n\t" \
  "v_cvt_pk_bf16_f32 v222, v188, v189\n\t" \
  "v_cvt_pk_bf16_f32 v223, v190, v191\n\t" \
  "v_mfma_f32_16x16x32_bf16 a[56:59], v[124:127], v[116:119], a[56:59]\n\t" \
  "ds_read_b64_tr_b16 v[144:145], v161 offset:16384\n\t" \
  "v_cvt_pk_bf16_f32 v224, v192, v193\n\t" \
  "v_mfma_f32_16x16x32_bf16 a[60:63], v[128:131], v[80:83], a[60:63]\n\t" \
  "v_cvt_pk_bf16_f32 v225, v194, v195\n\t" \
  "v_cvt_pk_bf16_f32 v226, v196, v197\n\t" \
  "v_mfma_f32_16x16x32_bf16 a[60:63], v[132:135], v[100:103], a[60:63]\n\t" \
  "ds_read_b64_tr_b16 v[146:147], v161 offset:20480\n\t" \
  "v_cvt_pk_bf16_f32 v227, v198, v199\n\t" \
  "v_mfma_f32_16x16x32_bf16 a[64:67], v[128:131], v[84:87], a[64:67]\n\t" \
  "v_cvt_pk_bf16_f32 v228, v200, v201\n\t" \
  "v_cvt_pk_bf16_f32 v229, v202, v203\n\t" \
  "v_mfma_f32_16x16x32_bf16 a[64:67], v[132:135], v[104:107], a[64:67]\n\t" \
  "ds_read_b64_tr_b16 v[148:149], v161 offset:24576\n\t" \
  "v_cvt_pk_bf16_f32 v230, v204, v205\n\t" \
  "v_mfma_f32_16x16x32_bf16 a[68:71], v[128:131], v[88:91], a[68:71]\n\t" \
  "v_cvt_pk_bf16_f32 v231, v206, v207\n\t" \
  "v_mfma_f32_16x16x32_bf16 a[68:71], v[132:135], v[108:111], a[68:71]\n\t" \
  "ds_read_b64_tr_b16 v[150:151], v161 offset:28672\n\t" \
  "v_mfma_f32_16x16x32_bf16 a[72:75], v[128:131], v[92:95], a[72:75]\n\t" \
  "v_mfma_f32_16x16x32_bf16 a[72:75], v[132:135], v[112:115], a[72:75]\n\t" \
  "global_load_dwordx4 a[168:171], v240, %[wfp] offset:2048\n\t" \
  "v_mfma_f32_16x16x32_bf16 a[76:79], v[128:131], v[96:99], a[76:79]\n\t" \
  "s_waitcnt lgkmcnt(4)\n\t" \
  "v_mfma_f32_16x16x32_bf16 a[76:79], v[132:135], v[116:119], a[76:79]\n\t" \
  "global_load_dwordx4 a[172:175], v240, %[wfp] offset:3072\n\t" \
  "v_mfma_f32_16x16x32_bf16 a[80:83], v[136:139], v[80:83], a[80:83]\n\t" \
  "ds_write_b128 v164, a[0:3] offset:0\n\t" \
  "v_mfma_f32_16x16x32_bf16 a[80:83], v[140:143], v[100:103], a[80:83]\n\t" \
  "ds_read_b64_tr_b16 v[120:121], v162 offset:16384\n\t" \
  "v_mfma_f32_16x16x32_bf16 a[84:87], v[136:139], v[84:87], a[84:87]\n\t" \
  "ds_write_b128 v164, a[4:7] offset:1024\n\t" \
  "v_mfma_f32_16x16x32_bf16 a[84:87], v[140:143], v[104:107], a[84:87]\n\t" \
  "ds_read_b64_tr_b16 v[122:123], v162 offset:20480\n\t" \
  "v_mfma_f32_16x16x32_bf16 a[88:91], v[136:139], v[88:91], a[88:91]\n\t" \
  "s_waitcnt lgkmcnt(4)\n\t" \
  "ds_write_b128 v164, a[8:11] offset:2048\n\t" \
  "v_mfma_f32_16x16x32_bf16 a[88:91], v[140:143], v[108:111], a[88:91]\n\t" \
  "ds_read_b64_tr_b16 v[124:125], v162 offset:24576\n\t" \
  "v_mfma_f32_16x16x32_bf16 a[92:95], v[136:139], v[92:95], a[92:95]\n\t" \
  "ds_write_b128 v164, a[12:15] offset:3072\n\t" \
  "v_mfma_f32_16x16x32_bf16 a[92:95], v[140:143], v[112:115], a[92:95]\n\t" \
  "ds_read_b64_tr_b16 v[126:127], v162 offset:28672\n\t" \
  "v_mfma_f32_16x16x32_bf16 a[96:99], v[136:139], v[96:99], a[96:99]\n\t" \
  "ds_write_b128 v164, a[16:19] offset:4096\n\t" \
  "v_mfma_f32_16x16x32_bf16 a[96:99], v[140:143], v[116:119], a[96:99]\n\t" \
  "ds_read_b64_tr_b16 v[128:129], v163 offset:16384\n\t" \
  "v_mfma_f32_16x16x32_bf16 a[100:103], v[144:147], v[80:83], a[100:103]\n\t" \
  "ds_write_b128 v164, a[20:23] offset:5120\n\t" \
  "v_mfma_f32_16x16x32_bf16 a[100:103], v[148:151], v[100:103], a[100:103]\n\t" \
  "ds_read_b64_tr_b16 v[130:131], v163 offset:20480\n\t" \
  "v_mfma_f32_16x16x32_bf16 a[104:107], v[144:147], v[84:87], a[104:107]\n\t" \
  "ds_write_b128 v164, a[24:27] offset:6144\n\t" \
  "v_mfma_f32_16x16x32_bf16 a[104:107], v[148:151], v[104:107], a[104:107]\n\t" \
  "ds_read_b64_tr_b16 v[132:133], v163 offset:24576\n\t" \
  "v_mfma_f32_16x16x32_bf16 a[108:111], v[144:147], v[88:91], a[108:111]\n\t" \
  "ds_write_b128 v164, a[28:31] offset:7168\n\t" \
  "v_mfma_f32_16x16x32_bf16 a[108:111], v[148:151], v[108:111], a[108:111]\n\t" \
  "ds_read_b64_tr_b16 v[134:135], v163 offset:28672\n\t" \
  "v_mfma_f32_16x16x32_bf16 a[112:115], v[144:147], v[92:95], a[112:115]\n\t" \
  "ds_write_b128 v164, a[32:35] offset:8192\n\t" \
  "v_mfma_f32_16x16x32_bf16 a[112:115], v[148:151], v[112:115], a[112:115]\n\t" \
  "global_load_dwordx4 a[176:179], v241, %[wfp] offset:0\n\t" \
  "v_mfma_f32_16x16x32_bf16 a[116:119], v[144:147], v[96:99], a[116:119]\n\t" \
  "s_waitcnt lgkmcnt(9)\n\t" \
  "ds_write_b128 v164, a[36:39] offset:9216\n\t" \
  "v_mfma_f32_16x16x32_bf16 a[116:119], v[148:151], v[116:119], a[116:119]\n\t" \
  "global_load_dwordx4 a[180:183], v241, %[wfp] offset:1024\n\t" \
  "v_mfma_f32_16x16x32_bf16 a[120:123], v[120:123], v[80:83], a[120:123]\n\t" \
  "ds_write_b128 v164, a[40:43] offset:10240\n\t" \
  "v_mfma_f32_16x16x32_bf16 a[120:123], v[124:127], v[100:103], a[120:123]\n\t" \
  "global_load_dwordx4 a[184:187], v241, %[wfp] offset:2048\n\t" \
  "v_mfma_f32_16x16x32_bf16 a[124:127], v[120:123], v[84:87], a[124:127]\n\t" \
  "ds_write_b128 v164, a[44:47] offset:11264\n\t" \
  "v_mfma_f32_16x16x32_bf16 a[124:127], v[124:127], v[104:107], a[124:127]\n\t" \
  "global_load_dwordx4 a[188:191], v241, %[wfp] offset:3072\n\t" \
  "v_mfma_f32_16x16x32_bf16 a[128:131], v[120:123], v[88:91], a[128:131]\n\t" \
  "s_waitcnt lgkmcnt(4)\n\t" \
  "ds_write_b128 v164, a[48:51] offset:12288\n\t" \
  "v_mfma_f32_16x16x32_bf16 a[128:131], v[124:127], v[108:111], a[128:131]\n\t" \
  "global_load_dwordx4 a[192:195], v242, %[wfp] offset:0\n\t" \
  "v_mfma_f32_16x16x32_bf16 a[132:135], v[120:123], v[92:95], a[132:135]\n\t" \
  "ds_write_b128 v164, a[52:55] offset:13312\n\t" \
  "v_mfma_f32_16x16x32_bf16 a[132:135], v[124:127], v[112:115], a[132:135]\n\t" \
  "global_load_dwordx4 a[196:199], v242, %[wfp] offset:1024\n\t" \
  "v_mfma_f32_16x16x32_bf16 a[136:139], v[120:123], v[96:99], a[136:139]\n\t" \
  "ds_write_b128 v164, a[56:59] offset:14336\n\t" \
  "v_mfma_f32_16x16x32_bf16 a[136:139], v[124:127], v[116:119], a[136:139]\n\t" \
  "global_load_dwordx4 a[200:203], v242, %[wfp] offset:2048\n\t" \
  "v_mfma_f32_16x16x32_bf16 a[140:143], v[128:131], v[80:83], a[140:143]\n\t" \
  "ds_write_b128 v164, a[60:63] offset:15360\n\t" \
  "v_mfma_f32_16x16x32_bf16 a[140:143], v[132:135], v[100:103], a[140:143]\n\t" \
  "global_load_dwordx4 a[204:207], v242, %[wfp] offset:3072\n\t" \
  "v_mfma_f32_16x16x32_bf16 a[144:147], v[128:131], v[84:87], a[144:147]\n\t" \
  "ds_write_b128 v165, a[64:67] offset:0\n\t" \
  "v_mfma_f32_16x16x32_bf16 a[144:147], v[132:135], v[104:107], a[144:147]\n\t" \
  "global_load_dwordx4 a[208:211], v243, %[wfp] offset:0\n\t" \
  "v_mfma_f32_16x16x32_bf16 a[148:151], v[128:131], v[88:91], a[148:151]\n\t" \
  "ds_write_b128 v165, a[68:71] offset:1024\n\t" \
  "v_mfma_f32_16x16x32_bf16 a[148:151], v[132:135], v[108:111], a[148:151]\n\t" \
  "global_load_dwordx4 a[212:215], v243, %[wfp] offset:1024\n\t" \
  "v_mfma_f32_16x16x32_bf16 a[152:155], v[128:131], v[92:95], a[152:155]\n\t" \
  "ds_write_b128 v165, a[72:75] offset:2048\n\t" \
  "v_mfma_f32_16x16x32_bf16 a[152:155], v[132:135], v[112:115], a[152:155]\n\t" \
  "global_load_dwordx4 a[216:219], v243, %[wfp] offset:2048\n\t" \
  "v_mfma_f32_16x16x32_bf16 a[156:159], v[128:131], v[96:99], a[156:159]\n\t" \
  "ds_write_b128 v165, a[76:79] offset:3072\n\t" \
  "v_mfma_f32_16x16x32_bf16 a[156:159], v[132:135], v[116:119], a[156:159]\n\t" \
  "global_load_dwordx4 a[220:223], v243, %[wfp] offset:3072\n\t" \
  "ds_write_b128 v168, v[216:219] offset:0\n\t" \
  "ds_write_b128 v168, v[220:223] offset:1024\n\t" \
  "ds_write_b128 v168, v[224:227] offset:2048\n\t" \
  "ds_write_b128 v168, v[228:231] offset:3072\n\t" \
  "s_waitcnt lgkmcnt(0)\n\t" \
  "s_barrier\n\t" \
  "ds_read_b128 v[0:3], v166 offset:0\n\t" \
  "ds_read_b128 v[4:7], v166 offset:1024\n\t" \
  "ds_read_b128 v[8:11], v166 offset:2048\n\t" \
  "ds_read_b128 v[12:15], v166 offset:3072\n\t" \
  "ds_read_b128 v[16:19], v166 offset:4096\n\t" \
  "ds_read_b128 v[20:23], v166 offset:5120\n\t" \
  "ds_read_b128 v[24:27], v166 offset:6144\n\t" \
  "ds_read_b128 v[28:31], v166 offset:7168\n\t" \
  "ds_read_b128 v[32:35], v166 offset:8192\n\t" \
  "ds_read_b128 v[36:39], v166 offset:9216\n\t" \
  "ds_read_b128 v[40:43], v166 offset:10240\n\t" \
  "ds_read_b128 v[44:47], v166 offset:11264\n\t" \
  "ds_read_b128 v[48:51], v166 offset:12288\n\t" \
  "ds_read_b128 v[52:55], v166 offset:13312\n\t" \
  "ds_read_b128 v[56:59], v166 offset:14336\n\t" \
  "ds_read_b128 v[60:63], v166 offset:15360\n\t" \
  "ds_read_b128 v[64:67], v167 offset:0\n\t" \
  "ds_read_b128 v[68:71], v167 offset:1024\n\t" \
  "ds_read_b128 v[72:75], v167 offset:2048\n\t" \
  "ds_read_b128 v[76:79], v167 offset:3072\n\t" \
  "v_accvgpr_read_b32 v80, a80\n\t" \
  "v_accvgpr_read_b32 v81, a81\n\t" \
  "v_accvgpr_read_b32 v82, a82\n\t" \
  "v_accvgpr_read_b32 v83, a83\n\t" \
  "v_accvgpr_read_b32 v84, a84\n\t" \
  "v_accvgpr_read_b32 v85, a85\n\t" \
  "v_accvgpr_read_b32 v86, a86\n\t" \
  "v_accvgpr_read_b32 v87, a87\n\t" \
  "v_accvgpr_read_b32 v88, a88\n\t" \
  "v_accvgpr_read_b32 v89, a89\n\t" \
  "v_accvgpr_read_b32 v90, a90\n\t" \
  "v_accvgpr_read_b32 v91, a91\n\t" \
  "v_accvgpr_read_b32 v92, a92\n\t" \
  "v_accvgpr_read_b32 v93, a93\n\t" \
  "v_accvgpr_read_b32 v94, a94\n\t" \
  "v_accvgpr_read_b32 v95, a95\n\t" \
  "v_accvgpr_read_b32 v96, a96\n\t" \
  "v_accvgpr_read_b32 v97, a97\n\t" \
  "v_accvgpr_read_b32 v98, a98\n\t" \
  "v_accvgpr_read_b32 v99, a99\n\t" \
  "v_accvgpr_read_b32 v100, a100\n\t" \
  "v_accvgpr_read_b32 v101, a101\n\t" \
  "v_accvgpr_read_b32 v102, a102\n\t" \
  "v_accvgpr_read_b32 v103, a103\n\t" \
  "v_accvgpr_read_b32 v104, a104\n\t" \
  "v_accvgpr_read_b32 v105, a105\n\t" \
  "v_accvgpr_read_b32 v106, a106\n\t" \
  "v_accvgpr_read_b32 v107, a107\n\t" \
  "v_accvgpr_read_b32 v108, a108\n\t" \
  "v_accvgpr_read_b32 v109, a109\n\t" \
  "v_accvgpr_read_b32 v110, a110\n\t" \
  "v_accvgpr_read_b32 v111, a111\n\t" \
  "v_accvgpr_read_b32 v112, a112\n\t" \
  "v_accvgpr_read_b32 v113, a113\n\t" \
  "v_accvgpr_read_b32 v114, a114\n\t" \
  "v_accvgpr_read_b32 v115, a115\n\t" \
  "v_accvgpr_read_b32 v116, a116\n\t" \
  "v_accvgpr_read_b32 v117, a117\n\t" \
  "v_accvgpr_read_b32 v118, a118\n\t" \
  "v_accvgpr_read_b32 v119, a119\n\t" \
  "v_accvgpr_read_b32 v120, a120\n\t" \
  "v_accvgpr_read_b32 v121, a121\n\t" \
  "v_accvgpr_read_b32 v122, a122\n\t" \
  "v_accvgpr_read_b32 v123, a123\n\t" \
  "v_accvgpr_read_b32 v124, a124\n\t" \
  "v_accvgpr_read_b32 v125, a125\n\t" \
  "v_accvgpr_read_b32 v126, a126\n\t" \
  "v_accvgpr_read_b32 v127, a127\n\t" \
  "v_accvgpr_read_b32 v128, a128\n\t" \
  "v_accvgpr_read_b32 v129, a129\n\t" \
  "v_accvgpr_read_b32 v130, a130\n\t" \
  "v_accvgpr_read_b32 v131, a131\n\t" \
  "v_accvgpr_read_b32 v132, a132\n\t" \
  "v_accvgpr_read_b32 v133, a133\n\t" \
  "v_accvgpr_read_b32 v134, a134\n\t" \
  "v_accvgpr_read_b32 v135, a135\n\t" \
  "v_accvgpr_read_b32 v136, a136\n\t" \
  "v_accvgpr_read_b32 v137, a137\n\t" \
  "v_accvgpr_read_b32 v138, a138\n\t" \
  "v_accvgpr_read_b32 v139, a139\n\t" \
  "v_accvgpr_read_b32 v140, a140\n\t" \
  "v_accvgpr_read_b32 v141, a141\n\t" \
  "v_accvgpr_read_b32 v142, a142\n\t" \
  "v_accvgpr_read_b32 v143, a143\n\t" \
  "v_accvgpr_read_b32 v144, a144\n\t" \
  "v_accvgpr_read_b32 v145, a145\n\t" \
  "v_accvgpr_read_b32 v146, a146\n\t" \
  "v_accvgpr_read_b32 v147, a147\n\t" \
  "v_accvgpr_read_b32 v148, a148\n\t" \
  "v_accvgpr_read_b32 v149, a149\n\t" \
  "v_accvgpr_read_b32 v150, a150\n\t" \
  "v_accvgpr_read_b32 v151, a151\n\t" \
  "v_accvgpr_read_b32 v152, a152\n\t" \
  "v_accvgpr_read_b32 v153, a153\n\t" \
  "v_accvgpr_read_b32 v154, a154\n\t" \
  "v_accvgpr_read_b32 v155, a155\n\t" \
  "v_accvgpr_read_b32 v156, a156\n\t" \
  "v_accvgpr_read_b32 v157, a157\n\t" \
  "v_accvgpr_read_b32 v158, a158\n\t" \
  "v_accvgpr_read_b32 v159, a159\n\t" \
  "3:\n\t" \
  "v_mbcnt_lo_u32_b32 v246, -1, 0\n\t" \
  "v_mbcnt_hi_u32_b32 v246, -1, v246\n\t" \
  "v_and_b32 v246, 15, v246\n\t" \
  "s_mul_i32 s83, s82, 80\n\t" \
  "v_add_u32 v246, s83, v246\n\t" \
  "s_mul_i32 s83, %[jv], 40\n\t" \
  "v_subrev_u32 v246, s83, v246\n\t" \
  "v_mov_b32 v248, 0x7ff00000\n\t" \
  "v_add_u32 v247, 0, v246\n\t" \
  "v_cmp_gt_u32 vcc, 40, v247\n\t" \
  "v_add_u32 v240, 0, v172\n\t" \
  "s_nop 0\n\t" \
  "v_cndmask_b32 v240, v240, v248, vcc\n\t" \
  "v_add_u32 v247, 16, v246\n\t" \
  "v_cmp_gt_u32 vcc, 40, v247\n\t" \
  "v_add_u32 v241, 4096, v172\n\t" \
  "s_nop 0\n\t" \
  "v_cndmask_b32 v241, v241, v248, vcc\n\t" \
  "v_add_u32 v247, 32, v246\n\t" \
  "v_cmp_gt_u32 vcc, 40, v247\n\t" \
  "v_add_u32 v242, 8192, v172\n\t" \
  "s_nop 0\n\t" \
  "v_cndmask_b32 v242, v242, v248, vcc\n\t" \
  "v_add_u32 v247, 48, v246\n\t" \
  "v_cmp_gt_u32 vcc, 40, v247\n\t" \
  "v_add_u32 v243, 12288, v172\n\t" \
  "s_nop 0\n\t" \
  "v_cndmask_b32 v243, v243, v248, vcc\n\t" \
  "v_add_u32 v247, 64, v246\n\t" \
  "v_cmp_gt_u32 vcc, 40, v247\n\t" \
  "v_add_u32 v244, 16384, v172\n\t" \
  "s_nop 0\n\t" \
  "v_cndmask_b32 v244, v244, v248, vcc\n\t" \
  "s_waitcnt lgkmcnt(10)\n\t" \
  "v_add_f32 v0, v0, v80\n\t" \
  "v_add_f32 v1, v1, v81\n\t" \
  "v_add_f32 v2, v2, v82\n\t" \
  "v_add_f32 v3, v3, v83\n\t" \
  "v_add_f32 v20, v20, v100\n\t" \
  "v_add_f32 v21, v21, v101\n\t" \
  "v_add_f32 v22, v22, v102\n\t" \
  "v_add_f32 v23, v23, v103\n\t" \
  "v_add_f32 v4, v4, v84\n\t" \
  "v_add_f32 v5, v5, v85\n\t" \
  "v_add_f32 v6, v6, v86\n\t" \
  "v_add_f32 v7, v7, v87\n\t" \
  "v_add_f32 v24, v24, v104\n\t" \
  "v_add_f32 v25, v25, v105\n\t" \
  "v_add_f32 v26, v26, v106\n\t" \
  "v_add_f32 v27, v27, v107\n\t" \
  "v_add_f32 v8, v8, v88\n\t" \
  "v_add_f32 v9, v9, v89\n\t" \
  "v_add_f32 v10, v10, v90\n\t" \
  "v_add_f32 v11, v11, v91\n\t" \
  "v_add_f32 v28, v28, v108\n\t" \
  "v_add_f32 v29, v29, v109\n\t" \
  "v_add_f32 v30, v30, v110\n\t" \
  "v_add_f32 v31, v31, v111\n\t" \
  "v_add_f32 v12, v12, v92\n\t" \
  "v_add_f32 v13, v13, v93\n\t" \
  "v_add_f32 v14, v14, v94\n\t" \
  "v_add_f32 v15, v15, v95\n\t" \
  "v_add_f32 v32, v32, v112\n\t" \
  "v_add_f32 v33, v33, v113\n\t" \
  "v_add_f32 v34, v34, v114\n\t" \
  "v_add_f32 v35, v35, v115\n\t" \
  "v_add_f32 v16, v16, v96\n\t" \
  "v_add_f32 v17, v17, v97\n\t" \
  "v_add_f32 v18, v18, v98\n\t" \
  "v_add_f32 v19, v19, v99\n\t" \
  "v_add_f32 v36, v36, v116\n\t" \
  "v_add_f32 v37, v37, v117\n\t" \
  "v_add_f32 v38, v38, v118\n\t" \
  "v_add_f32 v39, v39, v119\n\t" \
  "v_cvt_pk_bf16_f32 v176, v0, v1\n\t" \
  "v_cvt_pk_bf16_f32 v177, v2, v3\n\t" \
  "v_cvt_pk_bf16_f32 v178, v20, v21\n\t" \
  "v_cvt_pk_bf16_f32 v179, v22, v23\n\t" \
  "buffer_store_dwordx4 v[176:179], v240, %[srdx], s79 offen offset:0 sc1\n\t" \
  "ds_write_b128 v166, v[176:179] offset:0\n\t" \
  "v_cvt_pk_bf16_f32 v180, v4, v5\n\t" \
  "v_cvt_pk_bf16_f32 v181, v6, v7\n\t" \
  "v_cvt_pk_bf16_f32 v182, v24, v25\n\t" \
  "v_cvt_pk_bf16_f32 v183, v26, v27\n\t" \
  "buffer_store_dwordx4 v[180:183], v241, %[srdx], s79 offen offset:0 sc1\n\t" \
  "ds_write_b128 v166, v[180:183] offset:2048\n\t" \
  "v_cvt_pk_bf16_f32 v184, v8, v9\n\t" \
  "v_cvt_pk_bf16_f32 v185, v10, v11\n\t" \
  "v_cvt_pk_bf16_f32 v186, v28, v29\n\t" \
  "v_cvt_pk_bf16_f32 v187, v30, v31\n\t" \
  "buffer_store_dwordx4 v[184:187], v242, %[srdx], s79 offen offset:0 sc1\n\t" \
  "ds_write_b128 v166, v[184:187] offset:4096\n\t" \
  "v_cvt_pk_bf16_f32 v188, v12, v13\n\t" \
  "v_cvt_pk_bf16_f32 v189, v14, v15\n\t" \
  "v_cvt_pk_bf16_f32 v190, v32, v33\n\t" \
  "v_cvt_pk_bf16_f32 v191, v34, v35\n\t" \
  "buffer_store_dwordx4 v[188:191], v243, %[srdx], s79 offen offset:0 sc1\n\t" \
  "ds_write_b128 v166, v[188:191] offset:6144\n\t" \
  "v_cvt_pk_bf16_f32 v192, v16, v17\n\t" \
  "v_cvt_pk_bf16_f32 v193, v18, v19\n\t" \
  "v_cvt_pk_bf16_f32 v194, v36, v37\n\t" \
  "v_cvt_pk_bf16_f32 v195, v38, v39\n\t" \
  "buffer_store_dwordx4 v[192:195], v244, %[srdx], s79 offen offset:0 sc1\n\t" \
  "ds_write_b128 v166, v[192:195] offset:8192\n\t" \
  "s_waitcnt lgkmcnt(0)\n\t" \
  "v_add_f32 v40, v40, v120\n\t" \
  "v_add_f32 v41, v41, v121\n\t" \
  "v_add_f32 v42, v42, v122\n\t" \
  "v_add_f32 v43, v43, v123\n\t" \
  "v_add_f32 v60, v60, v140\n\t" \
  "v_add_f32 v61, v61, v141\n\t" \
  "v_add_f32 v62, v62, v142\n\t" \
  "v_add_f32 v63, v63, v143\n\t" \
  "v_add_f32 v44, v44, v124\n\t" \
  "v_add_f32 v45, v45, v125\n\t" \
  "v_add_f32 v46, v46, v126\n\t" \
  "v_add_f32 v47, v47, v127\n\t" \
  "v_add_f32 v64, v64, v144\n\t" \
  "v_add_f32 v65, v65, v145\n\t" \
  "v_add_f32 v66, v66, v146\n\t" \
  "v_add_f32 v67, v67, v147\n\t" \
  "v_add_f32 v48, v48, v128\n\t" \
  "v_add_f32 v49, v49, v129\n\t" \
  "v_add_f32 v50, v50, v130\n\t" \
  "v_add_f32 v51, v51, v131\n\t" \
  "v_add_f32 v68, v68, v148\n\t" \
  "v_add_f32 v69, v69, v149\n\t" \
  "v_add_f32 v70, v70, v150\n\t" \
  "v_add_f32 v71, v71, v151\n\t" \
  "v_add_f32 v52, v52, v132\n\t" \
  "v_add_f32 v53, v53, v133\n\t" \
  "v_add_f32 v54, v54, v134\n\t" \
  "v_add_f32 v55, v55, v135\n\t" \
  "v_add_f32 v72, v72, v152\n\t" \
  "v_add_f32 v73, v73, v153\n\t" \
  "v_add_f32 v74, v74, v154\n\t" \
  "v_add_f32 v75, v75, v155\n\t" \
  "v_add_f32 v56, v56, v136\n\t" \
  "v_add_f32 v57, v57, v137\n\t" \
  "v_add_f32 v58, v58, v138\n\t" \
  "v_add_f32 v59, v59, v139\n\t" \
  "v_add_f32 v76, v76, v156\n\t" \
  "v_add_f32 v77, v77, v157\n\t" \
  "v_add_f32 v78, v78, v158\n\t" \
  "v_add_f32 v79, v79, v159\n\t" \
  "v_cvt_pk_bf16_f32 v196, v40, v41\n\t" \
  "v_cvt_pk_bf16_f32 v197, v42, v43\n\t" \
  "v_cvt_pk_bf16_f32 v198, v60, v61\n\t" \
  "v_cvt_pk_bf16_f32 v199, v62, v63\n\t" \
  "buffer_store_dwordx4 v[196:199], v240, %[srdx], s79 offen offset:1024 sc1\n\t" \
  "ds_write_b128 v166, v[196:199] offset:1024\n\t" \
  "v_cvt_pk_bf16_f32 v200, v44, v45\n\t" \
  "v_cvt_pk_bf16_f32 v201, v46, v47\n\t" \
  "v_cvt_pk_bf16_f32 v202, v64, v65\n\t" \
  "v_cvt_pk_bf16_f32 v203, v66, v67\n\t" \
  "buffer_store_dwordx4 v[200:203], v241, %[srdx], s79 offen offset:1024 sc1\n\t" \
  "ds_write_b128 v166, v[200:203] offset:3072\n\t" \
  "v_cvt_pk_bf16_f32 v204, v48, v49\n\t" \
  "v_cvt_pk_bf16_f32 v205, v50, v51\n\t" \
  "v_cvt_pk_bf16_f32 v206, v68, v69\n\t" \
  "v_cvt_pk_bf16_f32 v207, v70, v71\n\t" \
  "buffer_store_dwordx4 v[204:207], v242, %[srdx], s79 offen offset:1024 sc1\n\t" \
  "ds_write_b128 v166, v[204:207] offset:5120\n\t" \
  "v_cvt_pk_bf16_f32 v208, v52, v53\n\t" \
  "v_cvt_pk_bf16_f32 v209, v54, v55\n\t" \
  "v_cvt_pk_bf16_f32 v210, v72, v73\n\t" \
  "v_cvt_pk_bf16_f32 v211, v74, v75\n\t" \
  "buffer_store_dwordx4 v[208:211], v243, %[srdx], s79 offen offset:1024 sc1\n\t" \
  "ds_write_b128 v166, v[208:211] offset:7168\n\t" \
  "v_cvt_pk_bf16_f32 v212, v56, v57\n\t" \
  "v_cvt_pk_bf16_f32 v213, v58, v59\n\t" \
  "v_cvt_pk_bf16_f32 v214, v76, v77\n\t" \
  "v_cvt_pk_bf16_f32 v215, v78, v79\n\t" \
  "buffer_store_dwordx4 v[212:215], v244, %[srdx], s79 offen offset:1024 sc1\n\t" \
  "ds_write_b128 v166, v[212:215] offset:9216\n\t" \
  "s_waitcnt vmcnt(0) lgkmcnt(0)\n\t" \
  "s_lshl_b32 s83, s82, 1\n\t" \
  "v_mbcnt_lo_u32_b32 v0, -1, 0\n\t" \
  "v_add_u32 v0, s83, v0\n\t" \
  "v_cmp_ne_u32 vcc, %[jv], v0\n\t" \
  "v_lshlrev_b32 v0, 6, v0\n\t" \
  "v_mov_b32 v1, 1\n\t" \
  "s_and_b64 vcc, vcc, 3\n\t" \
  "s_mov_b64 s[90:91], exec\n\t" \
  "s_mov_b64 exec, vcc\n\t" \
  "s_cbranch_execz 4f\n\t" \
  "global_atomic_add v0, v1, %[cntp] sc1\n\t" \
  "4:\n\t" \
  "s_mov_b64 exec, s[90:91]\n\t"
#define ATTN_CLOBBERS "v0", "v1", "v2", "v3", "v4", "v5", "v6", "v7", "v8", "v9", "v10", "v11", "v12", "v13", "v14", "v15", "v16", "v17", "v18", "v19", "v20", "v21", "v22", "v23", "v24", "v25", "v26", "v27", "v28", "v29", "v30", "v31", "v32", "v33", "v34", "v35", "v36", "v37", "v38", "v39", "v40", "v41", "v42", "v43", "v44", "v45", "v46", "v47", "v48", "v49", "v50", "v51", "v52", "v53", "v54", "v55", "v56", "v57", "v58", "v59", "v60", "v61", "v62", "v63", "v64", "v65", "v66", "v67", "v68", "v69", "v70", "v71", "v72", "v73", "v74", "v75", "v76", "v77", "v78", "v79", "v80", "v81", "v82", "v83", "v84", "v85", "v86", "v87", "v88", "v89", "v90", "v91", "v92", "v93", "v94", "v95", "v96", "v97", "v98", "v99", "v100", "v101", "v102", "v103", "v104", "v105", "v106", "v107", "v108", "v109", "v110", "v111", "v112", "v113", "v114", "v115", "v116", "v117", "v118", "v119", "v120", "v121", "v122", "v123", "v124", "v125", "v126", "v127", "v128", "v129", "v130", "v131", "v132", "v133", "v134", "v135", "v136", "v137", "v138", "v139", "v140", "v141", "v142", "v143", "v144", "v145", "v146", "v147", "v148", "v149", "v150", "v151", "v152", "v153", "v154", "v155", "v156", "v157", "v158", "v159", "v160", "v161", "v162", "v163", "v164", "v165", "v166", "v167", "v168", "v169", "v170", "v171", "v172", "v173", "v174", "v175", "v176", "v177", "v178", "v179", "v180", "v181", "v182", "v183", "v184", "v185", "v186", "v187", "v188", "v189", "v190", "v191", "v192", "v193", "v194", "v195", "v196", "v197", "v198", "v199", "v200", "v201", "v202", "v203", "v204", "v205", "v206", "v207", "v208", "v209", "v210", "v211", "v212", "v213", "v214", "v215", "v216", "v217", "v218", "v219", "v220", "v221", "v222", "v223", "v224", "v225", "v226", "v227", "v228", "v229", "v230", "v231", "v232", "v233", "v234", "v235", "v236", "v237", "v238", "v239", "v240", "v241", "v242", "v243", "v244", "v245", "v246", "v247", "v248", "v249", "v250", "v251", "a0", "a1", "a2", "a3", "a4", "a5", "a6", "a7", "a8", "a9", "a10", "a11", "a12", "a13", "a14", "a15", "a16", "a17", "a18", "a19", "a20", "a21", "a22", "a23", "a24", "a25", "a26", "a27", "a28", "a29", "a30", "a31", "a32", "a33", "a34", "a35", "a36", "a37", "a38", "a39", "a40", "a41", "a42", "a43", "a44", "a45", "a46", "a47", "a48", "a49", "a50", "a51", "a52", "a53", "a54", "a55", "a56", "a57", "a58", "a59", "a60", "a61", "a62", "a63", "a64", "a65", "a66", "a67", "a68", "a69", "a70", "a71", "a72", "a73", "a74", "a75", "a76", "a77", "a78", "a79", "a80", "a81", "a82", "a83", "a84", "a85", "a86", "a87", "a88", "a89", "a90", "a91", "a92", "a93", "a94", "a95", "a96", "a97", "a98", "a99", "a100", "a101", "a102", "a103", "a104", "a105", "a106", "a107", "a108", "a109", "a110", "a111", "a112", "a113", "a114", "a115", "a116", "a117", "a118", "a119", "a120", "a121", "a122", "a123", "a124", "a125", "a126", "a127", "a128", "a129", "a130", "a131", "a132", "a133", "a134", "a135", "a136", "a137", "a138", "a139", "a140", "a141", "a142", "a143", "a144", "a145", "a146", "a147", "a148", "a149", "a150", "a151", "a152", "a153", "a154", "a155", "a156", "a157", "a158", "a159", "a160", "a161", "a162", "a163", "a164", "a165", "a166", "a167", "a168", "a169", "a170", "a171", "a172", "a173", "a174", "a175", "a176", "a177", "a178", "a179", "a180", "a181", "a182", "a183", "a184", "a185", "a186", "a187", "a188", "a189", "a190", "a191", "a192", "a193", "a194", "a195", "a196", "a197", "a198", "a199", "a200", "a201", "a202", "a203", "a204", "a205", "a206", "a207", "a208", "a209", "a210", "a211", "a212", "a213", "a214", "a215", "a216", "a217", "a218", "a219", "a220", "a221", "a222", "a223", "a224", "a225", "a226", "a227", "a228", "a229", "a230", "a231", "a232", "a233", "a234", "a235", "a236", "a237", "a238", "a239", "s72", "s73", "s74", "s75", "s76", "s77", "s78", "s79", "s80", "s81", "s82", "s83", "s84", "s85", "s86", "s87", "s88", "s89", "s90", "s91", "memory", "scc", "vcc"
#define ATTN_GATHER 1

typedef unsigned short u16;
typedef float f32x4 __attribute__((ext_vector_type(4)));
typedef unsigned u32x4 __attribute__((ext_vector_type(4)));
typedef unsigned u32x2 __attribute__((ext_vector_type(2)));
typedef int i32x4 __attribute__((ext_vector_type(4)));
typedef __bf16 bf16x8 __attribute__((ext_vector_type(8)));
typedef __bf16 bf16x2 __attribute__((ext_vector_type(2)));
typedef float f32x2 __attribute__((ext_vector_type(2)));
typedef short s16x4 __attribute__((ext_vector_type(4)));
typedef unsigned u32x16 __attribute__((ext_vector_type(16)));
#define DI __device__ __forceinline__
#define LDSP(T, p) ((__attribute__((address_space(3))) T*)(p))

constexpr int D = 128;
constexpr int NN = 10000;
constexpr int EE = 320000;
constexpr int HROWS = 10240;
constexpr int NPROD = 32, EPP = EE / NPROD;
constexpr int NWIN = 1000;
constexpr float ALPHA = 0.09696137f;
constexpr float RSQ128 = 0.08838834764831845f;
constexpr float QSCALE = 0.08838834764831845f * 1.4426950408889634f;

constexpr size_t WS_H = 0;
constexpr size_t WS_Q = WS_H + (size_t)HROWS * 256;
constexpr size_t WS_WF = WS_Q + (size_t)640 * 4096;
constexpr size_t WS_OFF = WS_WF + 65536;
constexpr size_t WS_SORT = WS_OFF + (size_t)1024 * 32 * 8;
constexpr size_t WS_X = WS_SORT + (size_t)EE * 4 + 4096;
constexpr size_t XG = 4 * 4 * 40 * 256;
constexpr size_t WS_FLAG = WS_X + 63 * XG;

DI float bf2f(unsigned v) { return __uint_as_float(v << 16); }
DI float bflo(unsigned v) { return __uint_as_float(v << 16); }
DI float bfhi(unsigned v) { return __uint_as_float(v & 0xffff0000u); }
DI unsigned pk2(float lo, float hi) { f32x2 v = {lo, hi}; bf16x2 b = __builtin_convertvector(v, bf16x2); return __builtin_bit_cast(unsigned, b); }
DI float xsum(float v) {
  auto r = __builtin_amdgcn_permlane16_swap(__float_as_uint(v), __float_as_uint(v), false, false);
  v = __uint_as_float(r[0]) + __uint_as_float(r[1]);
  auto r2 = __builtin_amdgcn_permlane32_swap(__float_as_uint(v), __float_as_uint(v), false, false);
  return __uint_as_float(r2[0]) + __uint_as_float(r2[1]);
}
DI f32x4 mfma16(u32x4 a, u32x4 b, f32x4 c) { return __builtin_amdgcn_mfma_f32_16x16x32_bf16(__builtin_bit_cast(bf16x8, a), __builtin_bit_cast(bf16x8, b), c, 0, 0, 0); }
DI u32x4 pack8(f32x4 a, f32x4 b) { u32x4 r = {pk2(a[0], a[1]), pk2(a[2], a[3]), pk2(b[0], b[1]), pk2(b[2], b[3])}; return r; }
DI u32x4 sub4(const u32x16& v, int i) { return u32x4{v[4 * i], v[4 * i + 1], v[4 * i + 2], v[4 * i + 3]}; }
DI int prow(int T, int rho) { return 32 * (T >> 1) + 8 * (rho >> 2) + 4 * (T & 1) + (rho & 3); }

#define TL_BEGIN
#define TL_END(K, WSP)
#define KS(i)
constexpr int K1_ROWWG = 160, K1_WWG = 2, K1_NPOIS = 62;
#define POIS_AUX 16
#define POLLV 0
#define PREF_K1 1
#define K1_AUX 2
__global__ __launch_bounds__(512) void k_prep(const u16* __restrict__ x, const int* __restrict__ src, const int* __restrict__ dst,
                                              const u16* __restrict__ wm, const u16* __restrict__ wq, const u16* __restrict__ wk,
                                              const u16* __restrict__ wv, const u16* __restrict__ wo, char* __restrict__ ws) {
  extern __shared__ __attribute__((aligned(16))) char lds[];
  TL_BEGIN
  const int tid = threadIdx.x, lane = tid & 63, wv_ = tid >> 6, l15 = lane & 15, g = lane >> 4;
  const int b = blockIdx.x;
  u32x4 pf0, pf1;
  {
    const int o1 = tid * 16 + ((b >> 3) & 1) * 4096, o2 = (((b >> 3) % 5) * 512 + tid) * 16;
    asm volatile("s_getpc_b64 s[40:41]\n\tglobal_load_dwordx4 %0, %2, s[40:41]\n\t"
                 "s_getpc_b64 s[40:41]\n\ts_add_u32 s40, s40, _Z6k_mainPKtS0_PcPt@rel32@lo+4\n\ts_addc_u32 s41, s41, _Z6k_mainPKtS0_PcPt@rel32@hi+12\n\t"
                 "global_load_dwordx4 %1, %3, s[40:41]"
                 : "=&v"(pf0), "=&v"(pf1) : "v"(o1), "v"(o2) : "s40", "s41", "scc");
  }
  if (b < K1_ROWWG) {
    const bool hasrows = __builtin_amdgcn_readfirstlane(wv_) < 4;
    const int tile = b * 4 + (wv_ & 3), row = tile * 16 + l15;
    const bool valid = row < NN;
    const int rowc = valid ? row : NN - 1;
    u32x4 xr[4];
    if (hasrows) {
#pragma unroll
      for (int s = 0; s < 4; ++s) xr[s] = *(const u32x4*)(x + rowc * D + 32 * s + 8 * g);
    }
#pragma unroll
    for (int i = 0; i < 8; ++i) {
      const int piece = wv_ * 8 + i, r = (piece & 31) * 4 + g;
      const u16* wsrc = (piece < 32 ? wq : wk) + r * D + 8 * (l15 ^ ((((r >> 1) & 4) | (r & 3)) << 1));
      __builtin_amdgcn_global_load_lds((const __attribute__((address_space(1))) void*)wsrc, LDSP(void, lds + piece * 1024), 16, 0, 0);
    }
    __amdgpu_buffer_rsrc_t srdh = __builtin_amdgcn_make_buffer_rsrc((void*)(ws + WS_H), (short)0, HROWS * 256, 0x00020000);
    __amdgpu_buffer_rsrc_t srdq = __builtin_amdgcn_make_buffer_rsrc((void*)(ws + WS_Q), (short)0, 640 * 4096, 0x00020000);
    u32x4 hf[4];
    if (hasrows) {
      float h[32];
      float s1 = 0.f;
#pragma unroll
      for (int s = 0; s < 4; ++s)
#pragma unroll
        for (int e = 0; e < 4; ++e) { h[8 * s + 2 * e] = bflo(xr[s][e]); h[8 * s + 2 * e + 1] = bfhi(xr[s][e]); s1 += h[8 * s + 2 * e] + h[8 * s + 2 * e + 1]; }
      KS(1);
      const float mean = xsum(s1) * (1.f / 128.f);
      float s2 = 0.f;
#pragma unroll
      for (int i = 0; i < 32; ++i) { h[i] -= mean; s2 += h[i] * h[i]; }
      const float rstd = valid ? rsqrtf(xsum(s2) * (1.f / 128.f) + 1e-5f) : 0.f;
      const float h127 = xsum(g == 3 ? h[31] : 0.f) * rstd * ALPHA;
#pragma unroll
      for (int s = 0; s < 4; ++s) {
        u32x4 t;
#pragma unroll
        for (int e = 0; e < 4; ++e) {
          const float a = h[8 * s + 2 * e] * rstd, c = h[8 * s + 2 * e + 1] * rstd;
          hf[s][e] = pk2(a, c); t[e] = pk2(a + h127, c + h127);
        }
        if (s == 3 && g == 3) t[3] = (t[3] & 0xffffu) | (valid ? 0x3F800000u : 0u);
        __builtin_amdgcn_raw_buffer_store_b128(t, srdh, row * 256 + 16 * ((4 * s + g) ^ ((row & 7) << 1)), 0, K1_AUX);
      }
    }
    KS(2);
    if (hasrows) asm volatile("s_waitcnt vmcnt(4)" ::: "memory");
    else asm volatile("s_waitcnt vmcnt(0)" ::: "memory");
    __builtin_amdgcn_s_barrier();
    KS(3);
    {
      const int fx = (l15 & 7) << 1, a2 = wv_ >> 1, c4 = wv_ & 1;
      u32x4 qa[2][4];
#pragma unroll
      for (int u = 0; u < 2; ++u)
#pragma unroll
        for (int ke = 0; ke < 4; ++ke) qa[u][ke] = *(const u32x4*)(lds + prow(2 * a2 + u, l15) * 256 + 16 * ((4 * ke + g) ^ fx));
      u32x4 kf[4][4];
#pragma unroll
      for (int t = 0; t < 4; ++t)
#pragma unroll
        for (int ke = 0; ke < 4; ++ke) kf[t][ke] = *(const u32x4*)(lds + 32768 + prow(4 * c4 + t, l15) * 256 + 16 * ((4 * ke + g) ^ fx));
      f32x4 m0[4], m1[4];
#pragma unroll
      for (int t = 0; t < 4; ++t) { m0[t] = f32x4{0.f, 0.f, 0.f, 0.f}; m1[t] = f32x4{0.f, 0.f, 0.f, 0.f}; }
#pragma unroll
      for (int ke = 0; ke < 4; ++ke)
#pragma unroll
        for (int t = 0; t < 4; ++t) { m0[t] = mfma16(qa[0][ke], kf[t][ke], m0[t]); m1[t] = mfma16(qa[1][ke], kf[t][ke], m1[t]); }
#pragma unroll
      for (int t = 0; t < 4; ++t) *(u32x4*)(lds + 65536 + ((4 * c4 + t) * 4 + a2) * 1024 + lane * 16) = pack8(m0[t] * QSCALE, m1[t] * QSCALE);
    }
    KS(4);
    __syncthreads();
    KS(5);
    if (hasrows) {
      f32x4 qd[8];
#pragma unroll
      for (int T = 0; T < 8; ++T) {
        f32x4 acc = {0.f, 0.f, 0.f, 0.f};
#pragma unroll
        for (int m = 0; m < 4; ++m) acc = mfma16(*(const u32x4*)(lds + 65536 + (T * 4 + m) * 1024 + lane * 16), hf[m], acc);
        qd[T] = acc;
      }
      float qs = 0.f;
#pragma unroll
      for (int T = 0; T < 8; ++T) qs += (qd[T][0] + qd[T][1]) + (qd[T][2] + qd[T][3]);
      const float qsum = xsum(qs), q127 = xsum(g == 3 ? qd[7][3] : 0.f);
      const float cq = ALPHA * (qsum * RSQ128 - q127);
      u32x4 qf[4]; float n2 = 0.f;
#pragma unroll
      for (int T = 0; T < 8; ++T) {
        qd[T] -= cq;
        if (T == 7 && g == 3) qd[7][3] = 0.f;
        n2 += (qd[T][0] * qd[T][0] + qd[T][1] * qd[T][1]) + (qd[T][2] * qd[T][2] + qd[T][3] * qd[T][3]);
      }
#pragma unroll
      for (int s = 0; s < 4; ++s) qf[s] = pack8(qd[2 * s], qd[2 * s + 1]);
      const float mt = sqrtf(xsum(n2)) * (11.3137085f * 1.015f) + 0.5f;
      if (g == 3) qf[3][3] = (qf[3][3] & 0xffffu) | (pk2(0.f, -mt) & 0xffff0000u);
#pragma unroll
      for (int s = 0; s < 4; ++s) __builtin_amdgcn_raw_buffer_store_b128(qf[s], srdq, tile * 4096 + s * 1024 + lane * 16, 0, K1_AUX);
      KS(6);
    }
  } else if (b < K1_ROWWG + K1_WWG) {
    const int mat = b - K1_ROWWG;
    const u16* wa = mat == 0 ? wv : wm;
    if (mat == 0 && tid < 256) *(unsigned*)(ws + WS_FLAG + tid * 64) = 0u;
#pragma unroll
    for (int i = 0; i < 8; ++i) {
      const int piece = wv_ * 8 + i, r = (piece & 31) * 4 + g;
      const u16* wsrc = (piece < 32 ? wo : wa) + r * D + 8 * (l15 ^ ((r & 7) << 1));
      __builtin_amdgcn_global_load_lds((const __attribute__((address_space(1))) void*)wsrc, LDSP(void, lds + piece * 1024), 16, 0, 0);
    }
    asm volatile("s_waitcnt vmcnt(0)" ::: "memory");
    __builtin_amdgcn_s_barrier();
    u32x4 bfr[4];
#pragma unroll
    for (int ke = 0; ke < 4; ++ke) {
      const int e0 = 32 * ke + 8 * g + (l15 >> 2);
      const int cb = (32 * (wv_ >> 1) + 8 * (lane & 3) + 4 * (wv_ & 1)) * 2;
      const char* p0 = lds + e0 * 256 + (cb ^ ((e0 & 7) << 5));
      const char* p1 = lds + (e0 + 4) * 256 + (cb ^ (((e0 + 4) & 7) << 5));
      s16x4 lo = __builtin_amdgcn_ds_read_tr16_b64_v4i16(LDSP(s16x4, p0)), hi = __builtin_amdgcn_ds_read_tr16_b64_v4i16(LDSP(s16x4, p1));
      u32x2 a = __builtin_bit_cast(u32x2, lo), c = __builtin_bit_cast(u32x2, hi);
      bfr[ke] = u32x4{a[0], a[1], c[0], c[1]};
    }
    f32x4 wd[8]; float cs = 0.f;
#pragma unroll
    for (int T = 0; T < 8; ++T) {
      f32x4 acc = {0.f, 0.f, 0.f, 0.f};
#pragma unroll
      for (int ke = 0; ke < 4; ++ke) acc = mfma16(*(const u32x4*)(lds + 32768 + (16 * T + l15) * 256 + 16 * ((4 * ke + g) ^ ((l15 & 7) << 1))), bfr[ke], acc);
      wd[T] = acc; cs += (acc[0] + acc[1]) + (acc[2] + acc[3]);
    }
    const float csum = xsum(cs), w127 = xsum(g == 3 ? wd[7][3] : 0.f);
    const float cw = ALPHA * (csum * RSQ128 - w127);
#pragma unroll
    for (int m = 0; m < 4; ++m) {
      u32x4 f = pack8(wd[2 * m] - cw, wd[2 * m + 1] - cw);
      if (m == 3 && g == 3) f[3] &= 0xffffu;
      *(u32x4*)(ws + WS_WF + ((mat * 8 + wv_) * 4 + m) * 1024 + lane * 16) = f;
    }
  } else {
    const int p = b - K1_ROWWG - K1_WWG;
    unsigned* hist = (unsigned*)lds;
    unsigned* offs = (unsigned*)(lds + 4096);
    unsigned* wsum = (unsigned*)(lds + 8192);
    unsigned* sorted = (unsigned*)(lds + 8448);
    hist[tid] = 0; hist[tid + 512] = 0;
    i32x4 dv[5], sv[5];
#pragma unroll
    for (int i = 0; i < 5; ++i) { const int c = tid + 512 * i; const int cc = c < EPP / 4 ? c : 0;
      dv[i] = *(const i32x4*)(dst + p * EPP + cc * 4); sv[i] = *(const i32x4*)(src + p * EPP + cc * 4); }
    __syncthreads();
    unsigned wr[20];
#pragma unroll
    for (int i = 0; i < 5; ++i)
#pragma unroll
      for (int e = 0; e < 4; ++e) {
        const unsigned d = (unsigned)dv[i][e], w = (d * 52429u) >> 19;
        unsigned r = 0;
        if (tid + 512 * i < EPP / 4) r = atomicAdd(&hist[w], 1u);
        wr[4 * i + e] = (w << 16) | r;
        sv[i][e] = (sv[i][e] << 8) | ((sv[i][e] & 7) << 5) | (int)(d - 10u * w);
      }
    __syncthreads();
    const unsigned c0 = hist[2 * tid], c1 = hist[2 * tid + 1];
    unsigned inc = c0 + c1;
#pragma unroll
    for (int o = 1; o < 64; o <<= 1) { const unsigned t = __shfl_up(inc, o); if (lane >= o) inc += t; }
    if (lane == 63) wsum[wv_] = inc;
    __syncthreads();
    unsigned base = 0;
#pragma unroll
    for (int i = 0; i < 8; ++i) base += (i < wv_) ? wsum[i] : 0u;
    const unsigned ex = base + inc - c0 - c1;
    offs[2 * tid] = ex; offs[2 * tid + 1] = ex + c0;
    u32x2* off2 = (u32x2*)(ws + WS_OFF);
    if (2 * tid < NWIN + 8) { off2[(2 * tid) * 32 + p] = u32x2{p * EPP + ex, c0}; off2[(2 * tid + 1) * 32 + p] = u32x2{p * EPP + ex + c0, c1}; }
    __syncthreads();
#pragma unroll
    for (int i = 0; i < 5; ++i)
#pragma unroll
      for (int e = 0; e < 4; ++e) if (tid + 512 * i < EPP / 4) sorted[offs[wr[4 * i + e] >> 16] + (wr[4 * i + e] & 0xffffu)] = (unsigned)sv[i][e];
    __syncthreads();
    u32x4* out = (u32x4*)(ws + WS_SORT + (size_t)p * EPP * 4);
#pragma unroll
    for (int i = 0; i < 5; ++i) { const int c = tid + 512 * i; if (c < EPP / 4) out[c] = *(const u32x4*)(sorted + 4 * c); }
  }
  asm volatile("s_waitcnt vmcnt(0)" :: "v"(pf0), "v"(pf1));
  TL_END(0, ws);
}

typedef __attribute__((address_space(1))) unsigned gu32;
DI float dot2(unsigned a, unsigned k, float c) { return __builtin_amdgcn_fdot2_f32_bf16(__builtin_bit_cast(bf16x2, a), __builtin_bit_cast(bf16x2, k), c, false); }
__global__ __launch_bounds__(256, 1) void k_main(const u16* __restrict__ x, const u16* __restrict__ deg_inv, char* __restrict__ ws, u16* __restrict__ out) {
  extern __shared__ __attribute__((aligned(16))) char lds[];
  TL_BEGIN
  const int b = blockIdx.x, gq = (b >> 5) * 8 + (b & 7), j = (b >> 3) & 3;
  if (gq >= 63) return;
  const int tid = threadIdx.x, lane = tid & 63, l15 = lane & 15, g = lane >> 4;
  const int w = __builtin_amdgcn_readfirstlane(tid >> 6);
  const int qg = w & 1, kg = w >> 1;
  unsigned soff = (j * 2 + kg) * 20 * 16384 + qg * 8192 - 12288;
  const char* hb = ws + WS_H;
  __amdgpu_buffer_rsrc_t srd = __builtin_amdgcn_make_buffer_rsrc((void*)hb, (short)0, HROWS * 256, 0x00020000);
  __amdgpu_buffer_rsrc_t srdg = __builtin_amdgcn_make_buffer_rsrc((void*)(hb - 4096), (short)0, HROWS * 256 + 4096, 0x00020000);
  __amdgpu_buffer_rsrc_t srdx = __builtin_amdgcn_make_buffer_rsrc((void*)(ws + WS_X + (size_t)gq * XG), (short)0, (int)XG, 0x00020000);
  const char* qbase = ws + WS_Q + (size_t)(gq * 10 + qg * 5) * 4096;
  const int win0 = gq * 16 + j * 4, win = win0 + w;
  const char* offp = ws + WS_OFF + (size_t)win * 256;
  const char* sorted = ws + WS_SORT;
  const int n = l15 < 10 ? l15 : 9;
  const int row = win * 10 + n;
  const unsigned drow = (unsigned)(row < NN ? row : NN - 1) * 2u;
  const unsigned sout = 81920u + (unsigned)w * 4096u;
  const char* wfp = ws + WS_WF + w * 8192;
  const unsigned xo = (unsigned)(win0 * 10 + n) * 256u + 64u * w + 16u * g;
  const char* cntp = ws + WS_FLAG + gq * 256;
  const int pslice = (((b >> 3) * 4 + w) % 38);
  u32x16 W0, W1, W2, W3, XV;
  unsigned ovf;
  asm volatile(ATTN_ASM
    : [soff] "+s"(soff), [ovf] "=&s"(ovf), "={a[160:175]}"(W0), "={a[176:191]}"(W1), "={a[192:207]}"(W2), "={a[208:223]}"(W3), "={a[224:239]}"(XV)
    : [srd] "s"(srd), [srdg] "s"(srdg), [srdx] "s"(srdx), [wv] "s"(w), [jv] "s"(j),
      [sorted] "s"(sorted), [offp] "s"(offp), [qbase] "s"(qbase), [degp] "s"(deg_inv), [drow] "v"(drow), [wfp] "s"(wfp), [xp] "s"(x), [xo] "v"(xo), [cntp] "s"(cntp), [pslice] "s"(pslice)
    : ATTN_CLOBBERS);
  const char* H = ws + WS_H;
  const unsigned scr = (w < 2 ? 32768u : 65536u - 16384u) + (unsigned)w * 8192u;
  if (ovf || !ATTN_GATHER) {
    float* acc = (float*)(lds + scr);
    for (int i = lane; i < 16 * 128; i += 64) acc[i] = 0.f;
    {
      const u32x2* so = (const u32x2*)offp;
      for (int p = 0; p < 32; ++p) {
        const unsigned st = so[p][0], ln = so[p][1];
        for (unsigned i = 0; i < ln; ++i) {
          const unsigned e = ((const unsigned*)sorted)[st + i], s = e >> 8, dl = e & 15u;
          const unsigned v = *(const unsigned*)(H + (size_t)s * 256 + 16 * ((lane >> 2) ^ ((s & 7) << 1)) + 4 * (lane & 3));
          acc[dl * 128 + 2 * lane] += bflo(v); acc[dl * 128 + 2 * lane + 1] += bfhi(v);
        }
      }
    }
    const float sc = bf2f(*(const u16*)((const char*)deg_inv + drow));
#pragma unroll
    for (int m = 0; m < 4; ++m)
      *(u32x4*)(lds + sout + m * 1024 + lane * 16) = pack8(*(const f32x4*)(acc + l15 * 128 + 32 * m + 4 * g) * sc, *(const f32x4*)(acc + l15 * 128 + 32 * m + 16 + 4 * g) * sc);
  }
  f32x4 yl[4][2];
  asm volatile("s_waitcnt lgkmcnt(0)\n\ts_barrier" ::: "memory");
#pragma unroll
  for (int u = 0; u < 4; ++u) {
    f32x4 y0 = {0.f, 0.f, 0.f, 0.f}, y1 = {0.f, 0.f, 0.f, 0.f};
#pragma unroll
    for (int m = 0; m < 4; ++m) {
      const u32x4 bs = *(const u32x4*)(lds + 81920 + u * 4096 + m * 1024 + lane * 16);
      y0 = mfma16(sub4(W2, m), bs, y0); y1 = mfma16(sub4(W3, m), bs, y1);
    }
    yl[u][0] = y0; yl[u][1] = y1;
    asm volatile("" : "+v"(yl[u][0]), "+v"(yl[u][1]));
  }
  float o[4][8];
  unsigned pc0 = 0, pc1 = 0;
  {
    {
      const char* cnt = cntp + j * 64;
      if (w == 0) {
        unsigned zero = 0, spins = 1u << 20;
        while (__hip_atomic_load((gu32*)cnt, __ATOMIC_RELAXED, __HIP_MEMORY_SCOPE_AGENT) < 6u) { if (--spins == 0) break; __builtin_amdgcn_s_sleep(1); }
      }
      __builtin_amdgcn_fence(__ATOMIC_ACQUIRE, "wavefront");
      __builtin_amdgcn_s_barrier();
    }
    u32x4 pv[4][4];
    const int rho = 40 * j + 10 * w + n, tau = rho >> 4;
    const int xoff = tau * 4096 + (g * 16 + (rho & 15)) * 16;
#pragma unroll
    for (int js = 1; js < 4; ++js)
#pragma unroll
      for (int m = 0; m < 4; ++m) pv[js][m] = __builtin_amdgcn_raw_buffer_load_b128(srdx, xoff + ((j + js) & 3) * 40960 + m * 1024, 0, 16);
    { const int qg2 = tau >= 5 ? 1 : 0, qb2 = tau - 5 * qg2;
#pragma unroll
      for (int m = 0; m < 4; ++m) pv[0][m] = *(const u32x4*)(lds + 98304 + ((qg2 + 2 * (m >> 1)) ^ 2) * 16384 + (qb2 * 2 + (m & 1)) * 1024 + (g * 16 + (rho & 15)) * 16); }
    unsigned klo = 0x00003F80u, khi = 0x3F800000u;
    asm volatile("" : "+v"(klo), "+v"(khi));
#pragma unroll
    for (int m = 0; m < 4; ++m)
#pragma unroll
      for (int e = 0; e < 4; ++e) {
        o[m][2 * e] = dot2(pv[3][m][e], klo, dot2(pv[2][m][e], klo, dot2(pv[1][m][e], klo, bflo(pv[0][m][e]))));
        o[m][2 * e + 1] = dot2(pv[3][m][e], khi, dot2(pv[2][m][e], khi, dot2(pv[1][m][e], khi, bfhi(pv[0][m][e]))));
      }
  }
  asm volatile("" :: "v"(pc0), "v"(pc1), "v"(o[0][0]));
  float l = xsum(g == 3 ? o[3][7] : 0.f);
  const bool bad = (row < NN) && (l15 < 10) && !(l >= 0x1p-60f);
  unsigned long long badm = __ballot(bad && g == 0);
  if (badm) {
    float* sb = (float*)(lds + scr);
    while (badm) {
      const int nb = __builtin_ctzll(badm); badm &= badm - 1;
      const int r = win * 10 + nb;
      const u16* Qf = (const u16*)(ws + WS_Q + (size_t)(r >> 4) * 4096);
      for (int k = 0; k < 2; ++k) { const int d = 2 * lane + k; sb[d] = d == 127 ? 0.f : bf2f(Qf[(d >> 5) * 512 + (((d >> 3) & 3) * 16 + (r & 15)) * 8 + (d & 7)]); }
      auto score = [&](int key) { float s = 0.f; const char* hr = H + (size_t)key * 256;
        for (int ch = 0; ch < 16; ++ch) { const u32x4 v = *(const u32x4*)(hr + 16 * (ch ^ ((key & 7) << 1)));
          for (int e = 0; e < 4; ++e) s += sb[8 * ch + 2 * e] * bflo(v[e]) + sb[8 * ch + 2 * e + 1] * bfhi(v[e]); }
        return s; };
      float mx = -3.0e38f;
      for (int key = lane; key < NN; key += 64) mx = fmaxf(mx, score(key));
      for (int o_ = 1; o_ < 64; o_ <<= 1) mx = fmaxf(mx, __shfl_xor(mx, o_));
      float a0 = 0.f, a1 = 0.f;
      for (int k0 = 0; k0 < NN; k0 += 64) {
        const int key = k0 + lane;
        const float p = key < NN ? __builtin_amdgcn_exp2f(score(key) - mx) : 0.f;
        const float pb = bf2f(pk2(p, 0.f) & 0xffffu);
        for (int i = 0; i < 64 && k0 + i < NN; ++i) {
          const float pi = __shfl(pb, i); const int kk = k0 + i;
          const unsigned v = *(const unsigned*)(H + (size_t)kk * 256 + 16 * ((lane >> 2) ^ ((kk & 7) << 1)) + 4 * (lane & 3));
          a0 += pi * bflo(v); a1 += pi * bfhi(v);
        }
      }
      sb[128 + 2 * lane] = a0; sb[128 + 2 * lane + 1] = a1;
      if (n == nb && l15 < 10) {
#pragma unroll
        for (int m = 0; m < 4; ++m)
#pragma unroll
          for (int e = 0; e < 8; ++e) o[m][e] = sb[128 + 32 * m + (e < 4 ? 4 * g + e : 16 + 4 * g + e - 4)];
        l = sb[128 + 127];
      }
    }
  }
  const float il = __builtin_amdgcn_rcpf(l);
#pragma unroll
  for (int m = 0; m < 4; ++m)
    *(u32x4*)(lds + 16384u + (unsigned)w * 4096u + m * 1024 + lane * 16) =
        u32x4{pk2(o[m][0] * il, o[m][1] * il), pk2(o[m][2] * il, o[m][3] * il), pk2(o[m][4] * il, o[m][5] * il), pk2(o[m][6] * il, o[m][7] * il)};
  __syncthreads();
#pragma unroll
  for (int u = 0; u < 4; ++u) {
    f32x4 y0 = yl[u][0], y1 = yl[u][1];
#pragma unroll
    for (int m = 0; m < 4; ++m) {
      const u32x4 bo = *(const u32x4*)(lds + 16384 + u * 4096 + m * 1024 + lane * 16);
      y0 = mfma16(sub4(W0, m), bo, y0); y1 = mfma16(sub4(W1, m), bo, y1);
    }
    const int r = (win0 + u) * 10 + l15;
    const u32x4 xq = sub4(XV, u);
    if (l15 < 10 && r < NN)
      *(u32x4*)(out + r * D + 32 * w + 8 * g) = u32x4{pk2(bflo(xq[0]) + y0[0], bfhi(xq[0]) + y0[1]), pk2(bflo(xq[1]) + y0[2], bfhi(xq[1]) + y0[3]),
                                                      pk2(bflo(xq[2]) + y1[0], bfhi(xq[2]) + y1[1]), pk2(bflo(xq[3]) + y1[2], bfhi(xq[3]) + y1[3])};
  }
  TL_END(1, ws);
}

extern "C" void kernel_launch(void* const* d_in, const int* in_sizes, int n_in, void* d_out, int out_size, void* d_ws, size_t ws_size, hipStream_t stream) {
  const u16* x = (const u16*)d_in[0]; const int* src = (const int*)d_in[1]; const int* dst = (const int*)d_in[2];
  const u16* deg_inv = (const u16*)d_in[3]; const u16* wm = (const u16*)d_in[4]; const u16* wq = (const u16*)d_in[5];
  const u16* wk = (const u16*)d_in[6]; const u16* wv = (const u16*)d_in[7]; const u16* wo = (const u16*)d_in[8];
  char* ws = (char*)d_ws;
  (void)hipFuncSetAttribute((const void*)k_prep, hipFuncAttributeMaxDynamicSharedMemorySize, 98304);
  (void)hipFuncSetAttribute((const void*)k_main, hipFuncAttributeMaxDynamicSharedMemorySize, 163840);
  k_prep<<<K1_ROWWG + K1_WWG + NPROD, 512, 98304, stream>>>(x, src, dst, wm, wq, wk, wv, wo, ws);
  k_main<<<256, 256, 163840, stream>>>(x, deg_inv, ws, (u16*)d_out);
}
```
